```python
import torch

def _softmax(x, axis=-1):
    return torch.softmax(x, dim=axis)
def _gelu(x):
    return torch.nn.functional.gelu(x, approximate="tanh")
def _silu(x):
    return torch.nn.functional.silu(x)
def _sigmoid(x):
    return torch.sigmoid(x)
def _layernorm(x, axis=-1, eps=1e-5):
    m = x.mean(dim=axis, keepdim=True)
    v = x.var(dim=axis, keepdim=True, unbiased=False)
    return (x - m) / torch.sqrt(v + eps)
def _scatter_add(tgt, idx, src, axis=0):
    return tgt.clone().index_add_(axis, idx, src)
def _segment_sum(src, seg, n):
    out = torch.zeros((n,) + src.shape[1:], dtype=src.dtype)
    return out.index_add_(0, seg, src)
def _pad(x, pw):
    flat = tuple(v for pair in reversed(pw) for v in pair)
    return torch.nn.functional.pad(x, flat)

kernel_name = 'gated_mix__mha_block__ssm_conv_h3__prompt'

def setup_inputs():

    _w = (lambda a: (lambda t: t.to(torch.bfloat16) if t.is_floating_point() else t)(torch.as_tensor(a)))
    def _A_setup():
        B, S, D, H = 1, 2048, 576, 32
        return {
            "x": _w((lambda *s: torch.randn(*s, dtype=torch.bfloat16) * 0.05)(B, S, D)),
            "wqkv": _w((lambda *s: torch.randn(*s, dtype=torch.bfloat16) * 0.8)(D, 3 * D)),
            "wo": _w((lambda *s: torch.randn(*s, dtype=torch.bfloat16) * 0.05)(D, D)),
        }
    _A_in = _A_setup()

    def _B_setup():
        B, S, D = 1, 2048, 576
        ks = 4
        return {
            "q": _w((lambda *s: torch.randn(*s, dtype=torch.bfloat16) * 0.05)(B, S, D)),
            "k": _w((lambda *s: torch.randn(*s, dtype=torch.bfloat16) * 0.4)(B, S, D)),
            "v": _w((lambda *s: torch.randn(*s, dtype=torch.bfloat16) * 0.4)(B, S, D)),
            "shift_k": _w((lambda *s: torch.randn(*s, dtype=torch.bfloat16) * 0.4)(D, ks) * 0.5),
            "ssm_k": _w((lambda *s: torch.randn(*s, dtype=torch.bfloat16) * 1.6)(D, S) * (1.0 / S ** 0.5)),
            "ssm_D": _w((lambda *s: torch.randn(*s, dtype=torch.bfloat16) * 0.4)(D)),
        }
    _B_in = _B_setup()

    return {
        "A_x": _A_in["x"],
        "A_wqkv": _A_in["wqkv"],
        "A_wo": _A_in["wo"],
        "B_q": _B_in["q"],
        "B_k": _B_in["k"],
        "B_v": _B_in["v"],
        "B_shift_k": _B_in["shift_k"],
        "B_ssm_k": _B_in["ssm_k"],
        "B_ssm_D": _B_in["ssm_D"],
        "gate_w": _w((lambda *s: torch.randn(*s, dtype=torch.bfloat16) * 0.05)(576) * 16.0),
    }

def reference(A_x, A_wqkv, A_wo, B_q, B_k, B_v, B_shift_k, B_ssm_k, B_ssm_D, gate_w):
    def _A_ref(x, wqkv, wo):
        B, S, D = x.shape
        H = 32
        Dh = D // H
        qkv = (lambda x, *d: x.permute(*d))((x @ wqkv).reshape(B, S, 3, H, Dh), 2, 0, 3, 1, 4)
        q, k, v = qkv[0], qkv[1], qkv[2]
        scores = torch.einsum("bhqd,bhkd->bhqk", q, k) * (Dh ** -0.5)
        p = _softmax(scores, axis=-1)
        o = torch.einsum("bhqk,bhkd->bhqd", p, v)
        return (lambda x, *d: x.permute(*d))(o, 0, 2, 1, 3).reshape(B, S, D) @ wo

    def _B_ref(q, k, v, shift_k, ssm_k, ssm_D):
        B, S, D = q.shape
        ks = shift_k.shape[-1]
        k_pad = _pad(k, ((0, 0), (ks - 1, 0), (0, 0)))
        k_shift = torch.zeros_like(k)
        for t in range(ks):
            k_shift = k_shift + k_pad[:, t:t + S, :] * shift_k[:, ks - 1 - t][None, None, :]
        kv = k_shift * v
        L = 2 * S
        kv_pad = _pad(kv, ((0, 0), (0, S), (0, 0)))
        kf_pad = _pad(ssm_k, ((0, 0), (0, S)))
        kv_f = (lambda x, axes=(-2,-1): torch.fft.rfft2(x.float(), dim=axes))(kv_pad[:, None, :, :], axes=(1, 2))
        kf_f = (lambda x, axes=(-2,-1): torch.fft.rfft2(x.float(), dim=axes))(kf_pad[:, None, :], axes=(1, 2))
        kf_f = (lambda x, *d: x.permute(*d))(kf_f, 1, 2, 0)
        y_f = kv_f * kf_f[None, :, :, :]
        y = (lambda x, s, axes=(-2,-1): torch.fft.irfft2(x, s=s, dim=axes).to(torch.bfloat16))(y_f, (1, L), axes=(1, 2))[:, 0, :S, :]
        y = y + kv * ssm_D[None, None, :]
        return q * y

    x = A_x
    g = _sigmoid((x * gate_w).sum(-1, keepdims=True))
    a = _A_ref(x, A_wqkv, A_wo)
    b = _B_ref(x, B_k, B_v, B_shift_k, B_ssm_k, B_ssm_D)
    return g * a + (1 - g) * b
```

```cpp
#include <hip/hip_runtime.h>
#include <hip/hip_bf16.h>
#include <cstdint>
#include <type_traits>
using bf16x8 = __attribute__((ext_vector_type(8))) short;
using s16x4 = __attribute__((ext_vector_type(4))) short;
using f32x16 = __attribute__((ext_vector_type(16))) float;
using f32x4 = __attribute__((ext_vector_type(4))) float;
using f32x2 = __attribute__((ext_vector_type(2))) float;
using u32x4 = __attribute__((ext_vector_type(4))) unsigned;
using u32x2 = __attribute__((ext_vector_type(2))) unsigned;
using i32x4 = __attribute__((ext_vector_type(4))) int;
typedef __bf16 bf2 __attribute__((ext_vector_type(2)));
typedef short v4i16_t __attribute__((ext_vector_type(4)));
#define DI __device__ __forceinline__
#define LAS __attribute__((address_space(3)))
#define GAS __attribute__((address_space(1)))
typedef LAS char* ldsp;
#define SBAR() __builtin_amdgcn_sched_barrier(0)
#define PIN(x) asm volatile("" : "+v"(x))
#define FENCE() asm volatile("" ::: "memory")
#define MFMAS(a, b, c) __builtin_amdgcn_mfma_f32_16x16x32_bf16(a, b, c, 0, 0, 0)
#define MFMA16(a, b, c) __builtin_amdgcn_mfma_f32_32x32x16_bf16(a, b, c, 0, 0, 0)
#define MFMA8(a, b, c) __builtin_amdgcn_mfma_f32_32x32x8bf16_1k(a, b, c, 0, 0, 0)
#define BARRIER() asm volatile("s_waitcnt lgkmcnt(0)\n\ts_barrier" ::: "memory")
#define WAITVM(n) asm volatile("s_waitcnt vmcnt(" #n ")" ::: "memory")
#define WAITLDS() asm volatile("s_waitcnt lgkmcnt(0)" ::: "memory")
#define SC1 16

constexpr int S = 2048, D = 576;
constexpr int TILE_REC = 2432;
constexpr int HEAD_REC = 64 * TILE_REC;
constexpr int QTILE_REC = 1152;
constexpr float QSCALE = 0.23570226039551584f * 1.4426950408889634f;
constexpr unsigned SPIN_MAX = 1u << 16;
constexpr unsigned WS_E = 0, WS_FP0 = 256, WS_FEND = WS_FP0 + 64 * 128, WS_GR = WS_FEND + 256 * 128, WS_GATE = WS_GR + 3 * 16 * 64 * 8,
                   WS_Q = WS_GATE + S * 4, WS_KV = WS_Q + 32 * 64 * QTILE_REC, WS_KVT = WS_KV + 32 * HEAD_REC, WS_Z = WS_KVT + D * S * 2,
                   WS_O = WS_Z + D * S * 2, WS_CLK = WS_O + D * S * 2;
constexpr int SLOT = 34816;
constexpr int L_STG = 3 * SLOT;
constexpr int HCOPY = 4160, HREC = 8 * HCOPY, UREC = 4608;
constexpr int L_H_A = SLOT, L_U_A = L_STG + 36864, L_X_A = 0;
constexpr int L_PK = 0, L_PV = 37888, L_PO = L_PV + 36864, L_SH = L_PO + 36864, L_GX = L_SH + 1024, L_GW = L_GX + 36864;
constexpr int L_H_B2 = L_SH, L_U_B = L_H_B2 + HREC, L_X_B = L_PO;
constexpr int L_WO = 0, L_RED = 82944, L_RED2 = L_RED + 49152, L_Z = L_RED2 + 16384;
constexpr int L_MX = 158720, L_SYNC = L_MX + 4096;
static_assert(L_GW + 2048 <= L_MX && L_U_B + 3 * UREC <= L_MX && L_U_A + 2 * UREC <= L_MX && L_Z + 4 * 2304 <= L_MX && L_H_A + 2 * HREC <= L_STG, "lds");
enum { SY_SLOT0 = 0, SY_READY = 1, SY_XCH = 5, SY_CONVDONE = 9 };

DI unsigned cvtpk(float a, float b) { f32x2 v = {a, b}; bf2 r = __builtin_convertvector(v, bf2); return __builtin_bit_cast(unsigned, r); }
DI i32x4 mkrsrc(const void* p, unsigned bytes) { i32x4 r; r[0] = __builtin_amdgcn_readfirstlane((int)(unsigned)(size_t)p); r[1] = __builtin_amdgcn_readfirstlane((int)(((size_t)p) >> 32) & 0xffff); r[2] = (int)bytes; r[3] = 0x00020000; return r; }
DI void dma16(i32x4 rsrc, unsigned voff, unsigned soff, unsigned ldsaddr) {
    asm volatile("s_mov_b32 m0, %3\n\ts_nop 0\n\tbuffer_load_dwordx4 %0, %1, %2 offen lds" :: "v"(voff), "s"(rsrc), "s"(soff), "s"(ldsaddr) : "memory");
}
DI s16x4 trread(ldsp p) { return __builtin_bit_cast(s16x4, __builtin_amdgcn_ds_read_tr16_b64_v4i16((LAS v4i16_t*)p)); }
DI float bflo(unsigned u) { return __uint_as_float(u << 16); }
DI float bfhi(unsigned u) { return __uint_as_float(u & 0xffff0000u); }
DI float dot2sq(unsigned u, float c) { const bf2 v = __builtin_bit_cast(bf2, u); return __builtin_amdgcn_fdot2_f32_bf16(v, v, c, false); }
DI unsigned wave_umax(unsigned v) {
    v = max(v, (unsigned)__builtin_amdgcn_update_dpp(0, (int)v, 0x111, 0xf, 0xf, false));
    v = max(v, (unsigned)__builtin_amdgcn_update_dpp(0, (int)v, 0x112, 0xf, 0xf, false));
    v = max(v, (unsigned)__builtin_amdgcn_update_dpp(0, (int)v, 0x114, 0xf, 0xf, false));
    v = max(v, (unsigned)__builtin_amdgcn_update_dpp(0, (int)v, 0x118, 0xf, 0xf, false));
    v = max(v, (unsigned)__builtin_amdgcn_update_dpp(0, (int)v, 0x142, 0xa, 0xf, false));
    v = max(v, (unsigned)__builtin_amdgcn_update_dpp(0, (int)v, 0x143, 0xc, 0xf, false));
    return (unsigned)__builtin_amdgcn_readlane((int)v, 63);
}
DI unsigned ld_flag(const char* p) { return __hip_atomic_load((const GAS unsigned*)p, __ATOMIC_RELAXED, __HIP_MEMORY_SCOPE_AGENT); }
DI unsigned long long ld_gran(const char* p) { return __hip_atomic_load((const GAS unsigned long long*)p, __ATOMIC_RELAXED, __HIP_MEMORY_SCOPE_AGENT); }
DI void st_flag(char* p, unsigned v) { __hip_atomic_store((GAS unsigned*)p, v, __ATOMIC_RELAXED, __HIP_MEMORY_SCOPE_AGENT); }
DI void st_gran(char* p, unsigned long long v) { __hip_atomic_store((GAS unsigned long long*)p, v, __ATOMIC_RELAXED, __HIP_MEMORY_SCOPE_AGENT); }
DI void poll_flags(const char* p, bool on, unsigned tag) {
    for (unsigned spin = 0; spin < SPIN_MAX; ++spin) {
        const unsigned v = on ? ld_flag(p) : tag;
        if (__all(v == tag)) break;
        __builtin_amdgcn_s_sleep(2);
    }
    FENCE();
}
DI void lds_inc(ldsp lds, int w) { __hip_atomic_fetch_add((LAS unsigned*)(lds + L_SYNC + 4 * w), 1u, __ATOMIC_RELAXED, __HIP_MEMORY_SCOPE_WORKGROUP); }
DI void lds_wait(ldsp lds, int w, unsigned n) {
    for (unsigned spin = 0; spin < SPIN_MAX * 16; ++spin) {
        const unsigned v = __builtin_amdgcn_readfirstlane(__hip_atomic_load((LAS unsigned*)(lds + L_SYNC + 4 * w), __ATOMIC_RELAXED, __HIP_MEMORY_SCOPE_WORKGROUP));
        if (v >= n) break;
        __builtin_amdgcn_s_sleep(1);
    }
    FENCE();
}

struct Args { const char *x, *wqkv, *wo, *bk, *bv, *shk, *ssmk, *ssmd, *gw; char *out, *ws; };

#define STAMP(i) do { } while (0)

DI void conv_load_h(const Args& a, int ch, int hf, unsigned ldsh, int lane, int n0, int n1) {
    const i32x4 rh = mkrsrc(a.ssmk + ch * 4096, 4096);
#pragma unroll
    for (int n = n0; n < n1; ++n) { const int c = 4 * hf + (n >> 2), q = n & 3;
        dma16(rh, lane * 16 + 2 * c + q * 1024, 0, ldsh + c * HCOPY + 32 + q * 1024); }
}

DI void conv_pair(const Args& a, ldsp lds, int ch, int hf, int pair, int lh, int lu, int lx, bool wait_slot0, unsigned tag, int lane, int dg) {
    const int l15 = lane & 15, kq = lane >> 4;
    poll_flags(a.ws + WS_FP0 + ((lane & 7) * 8 + dg) * 128, lane < 8, tag);
    const auto rkt = __builtin_amdgcn_make_buffer_rsrc((void*)(a.ws + WS_KVT), 0, D * S * 2, 0x00020000);
    const u32x4 u0 = __builtin_amdgcn_raw_buffer_load_b128(rkt, lane * 16, ch * 4096 + hf * 2048, SC1);
    const u32x4 u1 = __builtin_amdgcn_raw_buffer_load_b128(rkt, lane * 16, ch * 4096 + hf * 2048 + 1024, SC1);
    const u32x4 h0 = *(const GAS u32x4*)(a.ssmk + ch * 4096);
    const float dco = bflo(*(const GAS unsigned short*)(a.ssmd + ch * 2));
    WAITVM(0);
    *(LAS u32x4*)(lds + lu + hf * 2048 + lane * 16) = u0;
    *(LAS u32x4*)(lds + lu + hf * 2048 + 1024 + lane * 16) = u1;
    if (hf == 0) { if (lane < 32) *(LAS u32x4*)(lds + lu + 4096 + lane * 16) = (u32x4){0u, 0u, 0u, 0u}; }
    if (lane < 8) {
        const int c = 4 * hf + (lane & 3), sh = 16 * (8 - c);
        const unsigned long long lo = ((unsigned long long)h0[1] << 32) | h0[0], hi = ((unsigned long long)h0[3] << 32) | h0[2];
        unsigned long long rl, rh_;
        if (sh >= 128) { rl = 0; rh_ = 0; } else if (sh > 64) { rl = 0; rh_ = lo << (sh - 64); } else if (sh == 64) { rl = 0; rh_ = lo; } else { rl = lo << sh; rh_ = (hi << sh) | (lo >> (64 - sh)); }
        u32x4 w = {(unsigned)rl, (unsigned)(rl >> 32), (unsigned)rh_, (unsigned)(rh_ >> 32)};
        if (lane < 4) w = (u32x4){0u, 0u, 0u, 0u};
        *(LAS u32x4*)(lds + lh + c * HCOPY + (lane >> 2) * 16) = w;
    }
    WAITLDS();
    lds_inc(lds, SY_READY + pair);
    lds_wait(lds, SY_READY + pair, 128);
    const ldsp pa = lds + lh + ((l15 + 1) & 7) * HCOPY + (((l15 + 1) >> 3) + kq) * 16 + hf * 256;
    const ldsp pb = lds + lu + (4064 - 3584) - 32 * l15 + 16 * kq + hf * 256;
    f32x4 acc[8];
#pragma unroll
    for (int c = 0; c < 8; ++c) acc[c] = (f32x4){0.f, 0.f, 0.f, 0.f};
    bf16x8 A0[8], B0[8], A1[8], B1[8];
#define CRD(A, B, k0, i) do { A[i] = *(const LAS bf16x8*)(pa + (k0) * 64 + (i) * 512); B[i] = *(const LAS bf16x8*)(pb + (k0) * 64 + (7 - (i)) * 512); } while (0)
#define CMM(A, B, m, e) do { acc[(m) + (e)] = MFMAS(A[m], B[e], acc[(m) + (e)]); } while (0)
#define CLASS(A, B, NA, NB, k1, pre) do { \
        _Pragma("unroll") for (int m = 0; m < 8; ++m) { \
            if (pre) { CRD(NA, NB, k1, m); } SBAR(); \
            _Pragma("unroll") for (int e = 0; e < 8 - m; ++e) CMM(A, B, m, e); \
            SBAR(); \
        } } while (0)
#pragma unroll
    for (int i = 0; i < 8; ++i) CRD(A0, B0, 0, i);
    SBAR();
    CLASS(A0, B0, A1, B1, 1, true);
    CLASS(A1, B1, A0, B0, 2, true);
    CLASS(A0, B0, A1, B1, 3, true);
    CLASS(A1, B1, A0, B0, 0, false);
    if (wait_slot0) lds_wait(lds, SY_SLOT0, 256);
    f32x4 own[4];
#pragma unroll
    for (int i = 0; i < 4; ++i) { const f32x4 give = hf ? acc[i] : acc[4 + i]; own[i] = hf ? acc[4 + i] : acc[i];
        *(LAS f32x4*)(lds + lx + hf * 4096 + i * 1024 + lane * 16) = give; }
    lds_inc(lds, SY_XCH + pair);
    lds_wait(lds, SY_XCH + pair, 128);
    const auto rz = __builtin_amdgcn_make_buffer_rsrc((void*)(a.ws + WS_Z), 0, D * S * 2, 0x00020000);
#pragma unroll
    for (int i = 0; i < 4; ++i) {
        const f32x4 y = own[i] + *(const LAS f32x4*)(lds + lx + (hf ^ 1) * 4096 + i * 1024 + lane * 16);
        const int t = 256 * (4 * hf + i) + 16 * l15 + 4 * kq;
        const u32x2 u = *(const LAS u32x2*)(lds + lu + 2 * (2044 - t));
        const u32x2 z = {cvtpk(y[0] + bfhi(u[1]) * dco, y[1] + bflo(u[1]) * dco), cvtpk(y[2] + bfhi(u[0]) * dco, y[3] + bflo(u[0]) * dco)};
        __builtin_amdgcn_raw_buffer_store_b64(z, rz, 2 * t, ch * 4096, SC1);
    }
    WAITLDS();
    lds_inc(lds, SY_CONVDONE);
}

extern "C" __global__ __launch_bounds__(512, 2) void gated_mix_fused(Args a) {
    extern __shared__ __attribute__((aligned(16))) char smem[];
    const ldsp lds = (ldsp)smem; const unsigned lds0 = (unsigned)(size_t)smem;
    const int tid = threadIdx.x, lane = tid & 63;
    const int wid = __builtin_amdgcn_readfirstlane(tid >> 6);
    const int b = blockIdx.x, xcd = b & 7, r = b >> 3;
    STAMP(0);
    const unsigned e_raw = ld_flag(a.ws + WS_E);
    if (tid < 16) *(LAS unsigned*)(lds + L_SYNC + 4 * tid) = 0u;
    unsigned tag;
    int nconv;
    if (r < 24) {
        const int jb = 8 * (xcd >> 2) + r / 3, mat = r % 3, hg = xcd & 3;
        nconv = 4 * 64;
        if (wid >= 4) {
            const int pw = wid - 4;
            const i32x4 rx = mkrsrc(a.x, S * D * 2), rw = mkrsrc(a.wqkv, D * 3 * D * 2);
            unsigned vx[2], vw[5];
#pragma unroll
            for (int par = 0; par < 2; ++par) vx[par] = (lane >> 3) * 1152 + (((lane & 7) ^ ((4 * par + (lane >> 4)) & 7)) * 16);
#pragma unroll
            for (int n = 0; n < 5; ++n) { const int q = 64 * (pw + 4 * n) + lane, rho = (q * 3641) >> 16, pc = q - 18 * rho;
                const int k = (rho & 32) + ((rho >> 4) & 1) * 16 + ((rho >> 2) & 1) * 8 + ((rho >> 3) & 1) * 4 + (rho & 3);
                vw[n] = k * 3456 + pc * 16; }
            const unsigned wbase = mat * 1152 + 288 * hg;
            auto chunk = [&](int c) {
                const unsigned slot = lds0 + (c & 3) * SLOT;
#pragma unroll
                for (int n = 0; n < 4; ++n) { const int i = 4 * pw + n; dma16(rx, vx[n & 1], (128 * jb + 8 * i) * 1152 + c * 128, slot + i * 1024); }
#pragma unroll
                for (int n = 0; n < 5; ++n) if (n < 4 || pw < 2) dma16(rw, vw[n], 64 * c * 3456 + wbase, slot + 16384 + (pw + 4 * n) * 1024);
            };
            const int pair = pw >> 1, hf = pw & 1, ch = 72 * xcd + 2 * r + pair;
            const unsigned ldsh = lds0 + L_H_A + pair * HREC;
            chunk(0); chunk(1); chunk(2);
            for (int c = 0; c < 7; ++c) {
                if (pw < 2) WAITVM(18); else WAITVM(16);
                BARRIER();
                if (c + 3 < 9) chunk(c + 3);
            }
            if (pair == 0) conv_load_h(a, ch, hf, ldsh, lane, 0, 16);
            if (pw < 2) WAITVM(25); else WAITVM(8);
            BARRIER();
            if (pair == 1) conv_load_h(a, ch, hf, ldsh, lane, 0, 16);
            WAITVM(16);
            BARRIER();
            tag = __builtin_amdgcn_readfirstlane(e_raw) + 1u;
            STAMP(1);
            conv_pair(a, lds, ch, hf, pair, L_H_A + pair * HREC, L_U_A + pair * UREC, L_X_A + pair * 8192, true, tag, lane, xcd);
            STAMP(2);
        } else {
            const int l15 = lane & 15, kq = lane >> 4, q4 = l15 >> 2, p4 = lane & 3;
            unsigned ax[2];
#pragma unroll
            for (int s = 0; s < 2; ++s) ax[s] = (32 * wid + l15) * 128 + (((4 * s + kq) ^ (l15 >> 1)) * 16);
            const unsigned aw = 16384 + (16 * (kq >> 1) + 4 * (kq & 1) + q4) * 288 + p4 * 8;
            f32x4 acc[2][9];
#pragma unroll
            for (int nt = 0; nt < 2; ++nt)
#pragma unroll
                for (int m = 0; m < 9; ++m) acc[nt][m] = (f32x4){0.f, 0.f, 0.f, 0.f};
            struct Fr { s16x4 lo[9], hi[9]; bf16x8 x0, x1; };
            Fr F0, F1;
#define RD_X(N, slot, s) do { N.x0 = *(const LAS bf16x8*)(slot + ax[s]); N.x1 = *(const LAS bf16x8*)(slot + ax[s] + 2048); } while (0)
#define RD_W(N, slot, s, m) do { N.lo[m] = trread(slot + aw + (s) * 9216 + (m) * 32); N.hi[m] = trread(slot + aw + (s) * 9216 + 2304 + (m) * 32); } while (0)
#define MM(C, m) do { const bf16x8 w_ = __builtin_shufflevector(C.lo[m], C.hi[m], 0, 1, 2, 3, 4, 5, 6, 7); \
                if constexpr (VMAT) { acc[0][m] = MFMAS(C.x0, w_, acc[0][m]); acc[1][m] = MFMAS(C.x1, w_, acc[1][m]); } \
                else { acc[0][m] = MFMAS(w_, C.x0, acc[0][m]); acc[1][m] = MFMAS(w_, C.x1, acc[1][m]); } PIN(acc[0][m]); PIN(acc[1][m]); } while (0)
#define HALF(C, N, slot, s) do { \
                RD_X(N, slot, s); RD_W(N, slot, s, 0); SBAR(); MM(C, 0); SBAR(); \
                RD_W(N, slot, s, 1); SBAR(); MM(C, 1); SBAR(); RD_W(N, slot, s, 2); SBAR(); MM(C, 2); SBAR(); \
                RD_W(N, slot, s, 3); SBAR(); MM(C, 3); SBAR(); RD_W(N, slot, s, 4); SBAR(); MM(C, 4); SBAR(); \
                RD_W(N, slot, s, 5); SBAR(); MM(C, 5); SBAR(); RD_W(N, slot, s, 6); SBAR(); MM(C, 6); SBAR(); \
                RD_W(N, slot, s, 7); SBAR(); MM(C, 7); SBAR(); RD_W(N, slot, s, 8); SBAR(); MM(C, 8); SBAR(); } while (0)
            auto gemm = [&](auto vm) {
                constexpr bool VMAT = decltype(vm)::value;
                {
                    BARRIER();
                    const ldsp slot = lds;
                    RD_X(F0, slot, 0);
#pragma unroll
                    for (int m = 0; m < 9; ++m) RD_W(F0, slot, 0, m);
                    SBAR();
                    HALF(F0, F1, slot, 1);
                }
#pragma unroll 1
                for (int c = 1; c < 9; ++c) {
                    BARRIER();
                    const ldsp slot = lds + (c & 3) * SLOT;
                    HALF(F1, F0, slot, 0);
                    HALF(F0, F1, slot, 1);
                }
                WAITLDS();
                lds_inc(lds, SY_SLOT0);
#pragma unroll
                for (int m = 0; m < 9; ++m) MM(F1, m);
            };
            if (mat == 2) gemm(std::true_type{}); else gemm(std::false_type{});
            STAMP(1);
            const ldsp stg = lds + L_STG + wid * 9216;
            const int g = kq;
            if (mat < 2) {
                const float sc = mat == 0 ? QSCALE : 1.f;
#pragma unroll
                for (int m = 0; m < 9; ++m) {
                    const int G = 4 * m + g, pr = (G * 57) >> 9, gm = G - 9 * pr;
#pragma unroll
                    for (int nt = 0; nt < 2; ++nt) {
                        const f32x4 v = acc[nt][m] * sc; const int row32 = 16 * nt + l15;
                        const unsigned w0 = cvtpk(v[0], v[1]), w1 = cvtpk(v[2], v[3]);
                        if (gm == 4) { *(LAS unsigned*)(stg + (2 * pr) * 1152 + 1024 + row32 * 4) = w0; *(LAS unsigned*)(stg + (2 * pr + 1) * 1152 + 1024 + row32 * 4) = w1; }
                        else { const int hb = gm > 4, q = gm - 5 * hb;
                            *(LAS u32x2*)(stg + (2 * pr + hb) * 1152 + (row32 + 32 * (q >> 1)) * 16 + (q & 1) * 8) = (u32x2){w0, w1}; }
                    }
                }
            } else {
#pragma unroll
                for (int m = 0; m < 9; ++m) {
                    const int c = 16 * m + l15, hl = (c * 3641) >> 16, d = c - 18 * hl;
#pragma unroll
                    for (int nt = 0; nt < 2; ++nt) { const f32x4 v = acc[nt][m];
                        *(LAS u32x2*)(stg + hl * 1152 + (nt * 2 + (g & 1)) * 288 + d * 16 + (g >> 1) * 8) = (u32x2){cvtpk(v[0], v[1]), cvtpk(v[2], v[3])}; }
                }
            }
            WAITLDS();
            const int tile = 4 * jb + wid;
            const auto rq = __builtin_amdgcn_make_buffer_rsrc((void*)(a.ws + WS_Q), 0, 32 * 64 * QTILE_REC, 0x00020000);
            const auto rkv = __builtin_amdgcn_make_buffer_rsrc((void*)(a.ws + WS_KV), 0, 32 * HEAD_REC, 0x00020000);
            unsigned nm[4] = {0u, 0u, 0u, 0u};
#pragma unroll
            for (int n = 0; n < 9; ++n) { const int pi = 64 * n + lane, hl = (pi * 911) >> 16, off = (pi - 72 * hl) * 16;
                const u32x4 v = *(const LAS u32x4*)(stg + pi * 16);
                if (mat == 0) __builtin_amdgcn_raw_buffer_store_b128(v, rq, ((8 * hg + hl) * 64 + tile) * QTILE_REC + off, 0, SC1);
                else __builtin_amdgcn_raw_buffer_store_b128(v, rkv, (8 * hg + hl) * HEAD_REC + tile * TILE_REC + (mat - 1) * 1152 + off, 0, SC1);
                const unsigned ss = __float_as_uint(dot2sq(v[0], dot2sq(v[1], 0.f)) + dot2sq(v[2], dot2sq(v[3], 0.f)));
                const int p0 = (64 * n) / 144, p1 = (64 * n + 63) / 144;
                if (p0 == p1) nm[p0] = max(nm[p0], ss);
                else { const bool up = pi >= 144 * p1; nm[p0] = max(nm[p0], up ? 0u : ss); nm[p1] = max(nm[p1], up ? ss : 0u); }
            }
            if (mat == 2 && lane < 8) __builtin_amdgcn_raw_buffer_store_b128((u32x4){0x3f803f80u, 0x3f803f80u, 0x3f803f80u, 0x3f803f80u}, rkv, (8 * hg + lane) * HEAD_REC + tile * TILE_REC + 2304, 0, SC1);
            const unsigned n0 = wave_umax(nm[0]), n1 = wave_umax(nm[1]), n2 = wave_umax(nm[2]), n3 = wave_umax(nm[3]);
            tag = __builtin_amdgcn_readfirstlane(e_raw) + 1u;
            WAITVM(0);
            if (lane < 4) { const unsigned nv = lane == 0 ? n0 : lane == 1 ? n1 : lane == 2 ? n2 : n3;
                st_gran(a.ws + WS_GR + ((mat * 16 + 4 * hg + lane) * 64 + tile) * 8, ((unsigned long long)tag << 32) | nv); }
            STAMP(2);
        }
    } else {
        const int sp = (r - 24) * 8 + xcd, dg = xcd, tq = r - 24;
        nconv = 6 * 64;
        const i32x4 rk = mkrsrc(a.bk, S * D * 2), rv = mkrsrc(a.bv, S * D * 2), rs = mkrsrc(a.shk + dg * 576, 576), rg = mkrsrc(a.gw, 1152), rx = mkrsrc(a.x, S * D * 2);
        for (int i = wid; i < 37; i += 8) { const int q = 64 * i + lane, row = (q * 7282) >> 16, p = q - 9 * row;
            unsigned vo = (tq > 0 ? row : row - 3) * 1152 + p * 16; if (row >= 259 || (tq == 0 && row < 3)) vo = 0x80000000u;
            dma16(rk, vo, (tq > 0 ? (256 * tq - 3) * 1152 : 0) + dg * 144, lds0 + L_PK + i * 1024); }
        for (int i = wid; i < 36; i += 8) { const int q = 64 * i + lane, row = (q * 7282) >> 16, p = q - 9 * row;
            dma16(rv, row * 1152 + p * 16, 256 * tq * 1152 + dg * 144, lds0 + L_PV + i * 1024); }
        if (wid == 0) dma16(rs, lane * 16, 0, lds0 + L_SH);
        if (wid == 1) { dma16(rg, lane * 16, 0, lds0 + L_GW); dma16(rg, lane * 16 + 1024, 0, lds0 + L_GW + 1024); }
        for (int i = wid; i < 36; i += 8) dma16(rx, lane * 16, 32 * sp * 1152 + i * 1024, lds0 + L_GX + i * 1024);
        WAITVM(0);
        BARRIER();
        STAMP(1);
        for (int n = tid; n < 1152; n += 512) {
            const int o = (n * 1821) >> 16, cp = n - 36 * o;
            const ldsp kp = lds + L_PK + o * 1152 + cp * 4, vp = lds + L_PV + o * 1152 + cp * 4;
            const u32x4 sh = *(const LAS u32x4*)(lds + L_SH + cp * 16);
            const f32x2 s0 = {bflo(sh[0]), bflo(sh[2])}, s1 = {bfhi(sh[0]), bfhi(sh[2])}, s2 = {bflo(sh[1]), bflo(sh[3])}, s3 = {bfhi(sh[1]), bfhi(sh[3])};
            f32x2 kk[11];
#pragma unroll
            for (int i = 0; i < 11; ++i) { const unsigned u = *(const LAS unsigned*)(kp + i * 144); kk[i] = (f32x2){bflo(u), bfhi(u)}; }
            f32x2 kv[8];
#pragma unroll
            for (int e = 0; e < 8; ++e) { const unsigned u = *(const LAS unsigned*)(vp + e * 144); const f32x2 vv = {bflo(u), bfhi(u)};
                kv[e] = (kk[e] * s3 + kk[e + 1] * s2 + kk[e + 2] * s1 + kk[e + 3] * s0) * vv; }
            u32x4 w0, w1;
#pragma unroll
            for (int i = 0; i < 4; ++i) { w0[i] = cvtpk(kv[7 - 2 * i][0], kv[6 - 2 * i][0]); w1[i] = cvtpk(kv[7 - 2 * i][1], kv[6 - 2 * i][1]); }
            const int pos = (31 - o) ^ (cp & 7);
            *(LAS u32x4*)(lds + L_PO + (2 * cp) * 512 + pos * 16) = w0;
            *(LAS u32x4*)(lds + L_PO + (2 * cp + 1) * 512 + pos * 16) = w1;
        }
        {
            const int row = tid >> 4, part = tid & 15; float s = 0.f;
#pragma unroll
            for (int i = 0; i < 18; ++i) { const unsigned xv = *(const LAS unsigned*)(lds + L_GX + row * 1152 + (i * 16 + part) * 4), wv = *(const LAS unsigned*)(lds + L_GW + (i * 16 + part) * 4);
                s += bflo(xv) * bflo(wv) + bfhi(xv) * bfhi(wv); }
#pragma unroll
            for (int o = 1; o < 16; o <<= 1) s += __shfl_xor(s, o);
            if (part == 0) __hip_atomic_store((GAS float*)(a.ws + WS_GATE) + 32 * sp + row, s, __ATOMIC_RELAXED, __HIP_MEMORY_SCOPE_AGENT);
        }
        BARRIER();
        const auto rkvt = __builtin_amdgcn_make_buffer_rsrc((void*)(a.ws + WS_KVT), 0, D * S * 2, 0x00020000);
        for (int pi = tid; pi < 2304; pi += 512) { const int ch = pi >> 5, pos = pi & 31;
            const u32x4 v = *(const LAS u32x4*)(lds + L_PO + ch * 512 + ((pos ^ ((ch >> 1) & 7)) * 16));
            __builtin_amdgcn_raw_buffer_store_b128(v, rkvt, (72 * dg + ch) * 4096 + (1792 - 256 * tq) * 2 + pos * 16, 0, SC1); }
        const int pair = wid >> 1, hf = wid & 1, ch = 72 * xcd + 48 + 3 * tq + pair;
        const int lh = pair < 2 ? pair * HREC : L_H_B2;
        FENCE();
        if (pair < 3) { conv_load_h(a, ch, hf, lds0 + lh, lane, 0, 16); WAITVM(16); } else WAITVM(0);
        BARRIER();
        tag = __builtin_amdgcn_readfirstlane(e_raw) + 1u;
        if (tid == 0) st_flag(a.ws + WS_FP0 + sp * 128, tag);
        STAMP(3);
        if (pair < 3) conv_pair(a, lds, ch, hf, pair, lh, L_U_B + pair * UREC, L_X_B + pair * 8192, false, tag, lane, xcd);
        STAMP(2);
    }

    const int hp = 4 * (xcd & 3) + (r & 3), j = 8 * (xcd >> 2) + (r >> 2);
    const int hd = wid >> 2, head = 2 * hp + hd, kq = wid & 3;
    const int r32 = lane & 31, hi = lane >> 5;
    const int rh = 4 * xcd + (r >> 3), cg = r & 7;
    f32x16 O[4];
    {
        unsigned nq, nk, nv;
        {
            const char* gq = a.ws + WS_GR + ((0 * 16 + hp) * 64 + 4 * j + (lane & 3)) * 8;
            const char* gk = a.ws + WS_GR + ((1 * 16 + hp) * 64 + lane) * 8;
            const char* gv = a.ws + WS_GR + ((2 * 16 + hp) * 64 + lane) * 8;
            unsigned long long vq = 0, vk = 0, vv = 0;
            for (unsigned spin = 0; spin < SPIN_MAX; ++spin) {
                vq = ld_gran(gq); vk = ld_gran(gk); vv = ld_gran(gv);
                if (__all((unsigned)(vq >> 32) == tag && (unsigned)(vk >> 32) == tag && (unsigned)(vv >> 32) == tag)) break;
                __builtin_amdgcn_s_sleep(2);
            }
            FENCE();
            nq = (unsigned)vq; nk = (unsigned)vk; nv = (unsigned)vv;
        }
        STAMP(4);
        const auto rkv = __builtin_amdgcn_make_buffer_rsrc((void*)(a.ws + WS_KV + (size_t)head * HEAD_REC), 0, HEAD_REC, 0x00020000);
        const auto rq = __builtin_amdgcn_make_buffer_rsrc((void*)(a.ws + WS_Q + ((size_t)head * 64 + 4 * j) * QTILE_REC), 0, 4 * QTILE_REC, 0x00020000);
        const unsigned offK = lane * 16, offE = 1024 + r32 * 4;
        const unsigned offV0 = r32 < 18 ? 1152 + hi * 288 + r32 * 16 : 1152 + 1152, offV1 = r32 < 18 ? offV0 + 576 : offV0;
        struct KV { bf16x8 km; unsigned ke; bf16x8 v0, v1; };
        auto tileoff = [&](int blk) { return (unsigned)((4 * (blk & 15) + kq) * TILE_REC); };
        auto loadK = [&](KV& kv, int blk) { const unsigned so = tileoff(blk);
            kv.km = __builtin_bit_cast(bf16x8, __builtin_amdgcn_raw_buffer_load_b128(rkv, offK, so, SC1));
            kv.ke = __builtin_amdgcn_raw_buffer_load_b32(rkv, offE, so, SC1); };
        auto loadV0 = [&](KV& kv, int blk) { kv.v0 = __builtin_bit_cast(bf16x8, __builtin_amdgcn_raw_buffer_load_b128(rkv, offV0, tileoff(blk), SC1)); };
        auto loadV1 = [&](KV& kv, int blk) { kv.v1 = __builtin_bit_cast(bf16x8, __builtin_amdgcn_raw_buffer_load_b128(rkv, offV1, tileoff(blk), SC1)); };
        KV A, B;
        loadK(A, j); loadV0(A, j); loadV1(A, j);
        bf16x8 qm[4]; s16x4 qe[4];
#pragma unroll
        for (int qb = 0; qb < 4; ++qb) {
            qm[qb] = __builtin_bit_cast(bf16x8, __builtin_amdgcn_raw_buffer_load_b128(rq, offK, qb * QTILE_REC, SC1));
            unsigned e = __builtin_amdgcn_raw_buffer_load_b32(rq, offE, qb * QTILE_REC, SC1); e = hi ? 0u : e;
            const u32x2 t = {e, 0u}; qe[qb] = __builtin_bit_cast(s16x4, t);
        }
        loadK(B, j + 1); loadV0(B, j + 1);
        auto kext = [&](const KV& kv) { const u32x2 t = {kv.ke, 0x3f803f80u}; return __builtin_bit_cast(s16x4, t); };
        f32x16 zero; for (int i = 0; i < 16; ++i) zero[i] = 0.f;
        {
            const float bq = 3.f * __uint_as_float(wave_umax(nq)), bk = 3.f * __uint_as_float(wave_umax(nk)), bv = __uint_as_float(wave_umax(nv));
            const bool safe = (bq * bk <= 4096.f) && (bv <= 1.2e24f);
            if (!safe) {
                float mx[4] = {-3.0e38f, -3.0e38f, -3.0e38f, -3.0e38f};
#pragma unroll 1
                for (int s = 0; s < 16; ++s) {
                    KV T; loadK(T, s);
                    const s16x4 ke = kext(T);
#pragma unroll
                    for (int qb = 0; qb < 4; ++qb) {
                        f32x16 Sx = MFMA16(T.km, qm[qb], zero); Sx = MFMA8(ke, qe[qb], Sx);
#pragma unroll
                        for (int i = 0; i < 16; ++i) mx[qb] = fmaxf(mx[qb], Sx[i]);
                    }
                }
#pragma unroll
                for (int qb = 0; qb < 4; ++qb) { mx[qb] = fmaxf(mx[qb], __shfl_xor(mx[qb], 32));
                    if (hi == 0) *(LAS float*)(lds + L_MX + ((hd * 4 + kq) * 128 + qb * 32 + r32) * 4) = mx[qb]; }
                BARRIER();
#pragma unroll
                for (int qb = 0; qb < 4; ++qb) {
                    float m = -3.0e38f;
#pragma unroll
                    for (int w = 0; w < 4; ++w) m = fmaxf(m, *(const LAS float*)(lds + L_MX + ((hd * 4 + w) * 128 + qb * 32 + r32) * 4));
                    const unsigned h1 = cvtpk(-m, 0.f) & 0xffffu; const float r1 = -m - bflo(h1);
                    const unsigned h2 = cvtpk(r1, 0.f) & 0xffffu; const float r2 = r1 - bflo(h2);
                    const unsigned h3 = cvtpk(r2, 0.f) & 0xffffu;
                    u32x2 t = __builtin_bit_cast(u32x2, qe[qb]); t[1] = hi ? h3 : (h1 | (h2 << 16)); qe[qb] = __builtin_bit_cast(s16x4, t);
                }
            }
        }
#pragma unroll
        for (int qb = 0; qb < 4; ++qb)
#pragma unroll
            for (int i = 0; i < 16; ++i) O[qb][i] = 0.f;
        f32x16 S0, S1;
        s16x4 keA = kext(A);
        S0 = MFMA16(A.km, qm[0], zero); S0 = MFMA8(keA, qe[0], S0);
        float T0, T1, T2, T3; u32x4 p0, p1;
        T0 = T1 = T2 = T3 = 0.f; p1 = (u32x4){0u, 0u, 0u, 0u}; p0 = p1;
#define EX4(Sc, i) do { T0 = __builtin_amdgcn_exp2f(Sc[i]); SBAR(); T1 = __builtin_amdgcn_exp2f(Sc[i + 1]); SBAR(); T2 = __builtin_amdgcn_exp2f(Sc[i + 2]); SBAR(); T3 = __builtin_amdgcn_exp2f(Sc[i + 3]); PIN(T0); PIN(T1); PIN(T2); PIN(T3); SBAR(); } while (0)
#define CV2(P, i) do { P[i] = cvtpk(T0, T1); SBAR(); P[i + 1] = cvtpk(T2, T3); PIN(P); SBAR(); } while (0)
#define TILE(Sc, Sn, KM, KE, qn, V0, qb, PV1, qp) do { \
        Sn = MFMA16(KM, qm[qn], zero); SBAR(); \
        CV2(p1, 2); EX4(Sc, 0); \
        Sn = MFMA8(KE, qe[qn], Sn); SBAR(); \
        CV2(p0, 0); EX4(Sc, 4); \
        O[qp] = MFMA16(PV1, __builtin_bit_cast(bf16x8, p1), O[qp]); SBAR(); \
        CV2(p0, 2); EX4(Sc, 8); \
        O[qb] = MFMA16(V0, __builtin_bit_cast(bf16x8, p0), O[qb]); SBAR(); \
        CV2(p1, 0); EX4(Sc, 12); \
    } while (0)
#define STEP(C, keC, N, keN, sb) do { \
        TILE(S0, S1, C.km, keC, 1, C.v0, 0, N.v1, 3); \
        loadV1(N, (sb) + 1); SBAR(); \
        TILE(S1, S0, C.km, keC, 2, C.v0, 1, C.v1, 0); \
        TILE(S0, S1, C.km, keC, 3, C.v0, 2, C.v1, 1); \
        loadK(C, (sb) + 2); SBAR(); \
        keN = kext(N); \
        TILE(S1, S0, N.km, keN, 0, C.v0, 3, C.v1, 2); \
        loadV0(C, (sb) + 2); SBAR(); \
    } while (0)
        B.v1 = A.v1; PIN(A.v0); PIN(A.v1); PIN(B.v1);
        s16x4 keB;
        const i32x4 rwo = mkrsrc(a.wo, D * D * 2);
        unsigned vwa, vwb;
        { const int q = 64 * wid + lane, row = (q * 7282) >> 16; vwa = row * 1152 + (q - 9 * row) * 16;
          const int q2 = 512 + lane, row2 = (q2 * 7282) >> 16; vwb = row2 * 1152 + (q2 - 9 * row2) * 16; }
        PIN(vwa); PIN(vwb);
        PIN(S0);
        STAMP(5);
#pragma unroll 1
        for (int it = 0; it < 8; ++it) {
            if (it >= 2) {
                if (it == 2) lds_wait(lds, SY_CONVDONE, nconv);
#pragma unroll
                for (int u = 0; u < 2; ++u) { const int n = 2 * (it - 2) + u;
                    if (n < 9) dma16(rwo, vwa, n * 73728 + cg * 144, lds0 + L_WO + (9 * n + wid) * 1024);
                    else if (n == 9) dma16(rwo, vwb, wid * 73728 + cg * 144, lds0 + L_WO + (9 * wid + 8) * 1024);
                    else if (n == 10 && wid == 0) dma16(rwo, vwb, 8 * 73728 + cg * 144, lds0 + L_WO + 80 * 1024);
                }
            }
            const int s = 2 * it;
            STEP(A, keA, B, keB, j + s);
            STEP(B, keB, A, keA, j + s + 1);
        }
        { CV2(p1, 2); O[3] = MFMA16(B.v1, __builtin_bit_cast(bf16x8, p1), O[3]); }
        STAMP(6);
    }
    {
        f32x4 a0, a1;
#pragma unroll
        for (int qb = 0; qb < 4; ++qb) {
            const f32x4 x0 = {O[qb][0], O[qb][1], O[qb][2], O[qb][3]}, x1 = {O[qb][4], O[qb][5], O[qb][6], O[qb][7]}, x2 = {O[qb][8], O[qb][9], O[qb][10], O[qb][11]};
            if (hi == 0) *(LAS f32x4*)(lds + L_RED2 + ((hd * 4 + qb) * 4 + kq) * 512 + r32 * 16) = x2;
            if (qb == kq) { a0 = x0; a1 = x1; }
            else { const ldsp p = lds + L_RED + ((hd * 4 + qb) * 3 + (((kq - qb) & 3) - 1)) * 2048 + lane * 16; *(LAS f32x4*)p = x0; *(LAS f32x4*)(p + 1024) = x1; }
        }
        WAITVM(0);
        BARRIER();
        f32x4 a2 = {0.f, 0.f, 0.f, 0.f};
#pragma unroll
        for (int w = 0; w < 3; ++w) { const ldsp p = lds + L_RED + ((hd * 4 + kq) * 3 + w) * 2048 + lane * 16; a0 += *(const LAS f32x4*)p; a1 += *(const LAS f32x4*)(p + 1024); }
#pragma unroll
        for (int w = 0; w < 4; ++w) a2 += *(const LAS f32x4*)(lds + L_RED2 + ((hd * 4 + kq) * 4 + w) * 512 + r32 * 16);
        const float inv = 1.f / a2[2];
        const u32x4 om = {cvtpk(a0[0] * inv, a0[1] * inv), cvtpk(a0[2] * inv, a0[3] * inv), cvtpk(a1[0] * inv, a1[1] * inv), cvtpk(a1[2] * inv, a1[3] * inv)};
        const unsigned oe = cvtpk(a2[0] * inv, a2[1] * inv);
        const auto ro = __builtin_amdgcn_make_buffer_rsrc((void*)(a.ws + WS_O), 0, D * S * 2, 0x00020000);
        const unsigned rowbase = (8 * j + 2 * kq + (r32 >> 4)) * 18432 + (r32 & 15) * 16;
        __builtin_amdgcn_raw_buffer_store_b128(om, ro, rowbase + hp * 1024 + (2 * hd + hi) * 256, 0, SC1);
        if (hi == 0) __builtin_amdgcn_raw_buffer_store_b32(oe, ro, rowbase + (16 + (head >> 4)) * 1024 + ((head >> 2) & 3) * 256 + (head & 3) * 4, 0, SC1);
        WAITVM(0);
        BARRIER();
        if (tid == 0) st_flag(a.ws + WS_FEND + b * 128, tag);
        STAMP(7);
    }
    if (wid < 4) {
        const int l15 = lane & 15, g4 = lane >> 4, q4 = l15 >> 2, p4 = lane & 3;
        const int t0 = 64 * rh + 16 * wid;
        const auto rxx = __builtin_amdgcn_make_buffer_rsrc((void*)a.x, 0, S * D * 2, 0x00020000);
        u32x2 xv[5];
        const unsigned xo = (t0 + l15) * 1152 + cg * 144 + g4 * 8;
#pragma unroll
        for (int m = 0; m < 5; ++m) xv[m] = __builtin_amdgcn_raw_buffer_load_b64(rxx, (m < 4 || g4 < 2) ? xo + m * 32 : 0x80000000u, 0, 0);
        {
            const int jj = rh >> 1;
            int pb;
            if (lane < 16) pb = (4 * (jj >> 3) + (lane >> 2)) + 8 * (4 * (jj & 7) + (lane & 3));
            else if (lane < 48) pb = cg + 8 * (lane - 16);
            else { const int sp = 2 * rh + (lane & 1); pb = (sp & 7) + 8 * (24 + (sp >> 3)); }
            poll_flags(a.ws + WS_FEND + pb * 128, lane < 50, tag);
        }
        STAMP(8);
        const auto ro = __builtin_amdgcn_make_buffer_rsrc((void*)(a.ws + WS_O), 0, D * S * 2, 0x00020000);
        const auto rz = __builtin_amdgcn_make_buffer_rsrc((void*)(a.ws + WS_Z), 0, D * S * 2, 0x00020000);
        bf16x8 of[18];
        u32x4 zr[3];
#pragma unroll
        for (int n = 0; n < 3; ++n) { const int idx = 64 * n + lane;
            zr[n] = __builtin_amdgcn_raw_buffer_load_b128(rz, idx < 144 ? (72 * cg + (idx >> 1)) * 4096 + t0 * 2 + (idx & 1) * 16 : 0x80000000u, 0, SC1); }
        const float logit = __hip_atomic_load((const GAS float*)(a.ws + WS_GATE) + t0 + l15, __ATOMIC_RELAXED, __HIP_MEMORY_SCOPE_AGENT);
#pragma unroll
        for (int s = 0; s < 18; ++s) of[s] = __builtin_bit_cast(bf16x8, __builtin_amdgcn_raw_buffer_load_b128(ro, lane * 16, ((4 * rh + wid) * 18 + s) * 1024, SC1));
        const ldsp zl = lds + L_Z + wid * 2304;
#pragma unroll
        for (int n = 0; n < 3; ++n) if (n < 2 || lane < 16) *(LAS u32x4*)(zl + (64 * n + lane) * 16) = zr[n];
        f32x4 acc[5];
#pragma unroll
        for (int m = 0; m < 5; ++m) acc[m] = (f32x4){0.f, 0.f, 0.f, 0.f};
        const ldsp wm = lds + L_WO + (18 * (g4 >> 1) + 4 * (g4 & 1) + q4) * 144 + p4 * 8;
        const ldsp we = lds + L_WO + (18 * (4 * g4 + (q4 >> 1)) + 16 + (q4 & 1)) * 144 + p4 * 8;
#pragma unroll
        for (int s = 0; s < 18; ++s) {
#pragma unroll
            for (int m = 0; m < 5; ++m) {
                s16x4 lo, hh;
                if (s < 16) { lo = trread(wm + s * 36 * 144 + m * 32); hh = trread(wm + s * 36 * 144 + 8 * 144 + m * 32); }
                else { lo = trread(we + (s - 16) * 288 * 144 + m * 32); hh = trread(we + (s - 16) * 288 * 144 + 36 * 144 + m * 32); }
                acc[m] = MFMAS(__builtin_shufflevector(lo, hh, 0, 1, 2, 3, 4, 5, 6, 7), of[s], acc[m]);
            }
        }
        const float gt = 1.f / (1.f + __expf(-logit)), gn = 1.f - gt;
        const auto rout = __builtin_amdgcn_make_buffer_rsrc((void*)a.out, 0, S * D * 2, 0x00020000);
#pragma unroll
        for (int m = 0; m < 5; ++m) {
            const u32x2 z = __builtin_bit_cast(u32x2, trread(zl + (16 * m + 4 * g4 + q4) * 32 + p4 * 8));
            const float o0 = gt * acc[m][0] + gn * bflo(xv[m][0]) * bflo(z[0]), o1 = gt * acc[m][1] + gn * bfhi(xv[m][0]) * bfhi(z[0]);
            const float o2 = gt * acc[m][2] + gn * bflo(xv[m][1]) * bflo(z[1]), o3 = gt * acc[m][3] + gn * bfhi(xv[m][1]) * bfhi(z[1]);
            __builtin_amdgcn_raw_buffer_store_b64((u32x2){cvtpk(o0, o1), cvtpk(o2, o3)}, rout, (m < 4 || g4 < 2) ? xo + m * 32 : 0x80000000u, 0, 0);
        }
        STAMP(9);
        if (b == 0 && wid == 0) {
#pragma unroll 1
            for (int n = 0; n < 4; ++n) poll_flags(a.ws + WS_FEND + (64 * n + lane) * 128, true, tag);
            if (lane == 0) st_flag(a.ws + WS_E, tag);
        }
    }
}

extern "C" void kernel_launch(void* const* d_in, const int* in_sizes, int n_in, void* d_out, int out_size, void* d_ws, size_t ws_size, hipStream_t stream) {
    Args a;
    a.x = (const char*)d_in[0]; a.wqkv = (const char*)d_in[1]; a.wo = (const char*)d_in[2];
    a.bk = (const char*)d_in[4]; a.bv = (const char*)d_in[5]; a.shk = (const char*)d_in[6];
    a.ssmk = (const char*)d_in[7]; a.ssmd = (const char*)d_in[8]; a.gw = (const char*)d_in[9];
    a.out = (char*)d_out; a.ws = (char*)d_ws;
    (void)hipFuncSetAttribute((const void*)gated_mix_fused, hipFuncAttributeMaxDynamicSharedMemorySize, 163840);
    gated_mix_fused<<<256, 512, 163840, stream>>>(a);
}
```

```cpp
#include <hip/hip_runtime.h>
#include <hip/hip_bf16.h>
#include <cstdint>
#include <type_traits>
using bf16x8 = __attribute__((ext_vector_type(8))) short;
using s16x4 = __attribute__((ext_vector_type(4))) short;
using f32x16 = __attribute__((ext_vector_type(16))) float;
using f32x4 = __attribute__((ext_vector_type(4))) float;
using f32x2 = __attribute__((ext_vector_type(2))) float;
using u32x4 = __attribute__((ext_vector_type(4))) unsigned;
using u32x2 = __attribute__((ext_vector_type(2))) unsigned;
using i32x4 = __attribute__((ext_vector_type(4))) int;
typedef __bf16 bf2 __attribute__((ext_vector_type(2)));
typedef short v4i16_t __attribute__((ext_vector_type(4)));
#define DI __device__ __forceinline__
#define LAS __attribute__((address_space(3)))
#define GAS __attribute__((address_space(1)))
typedef LAS char* ldsp;
#define SBAR() __builtin_amdgcn_sched_barrier(0)
#define PIN(x) asm volatile("" : "+v"(x))
#define FENCE() asm volatile("" ::: "memory")
#define MFMAS(a, b, c) __builtin_amdgcn_mfma_f32_16x16x32_bf16(a, b, c, 0, 0, 0)
#define MFMA16(a, b, c) __builtin_amdgcn_mfma_f32_32x32x16_bf16(a, b, c, 0, 0, 0)
#define MFMA8(a, b, c) __builtin_amdgcn_mfma_f32_32x32x8bf16_1k(a, b, c, 0, 0, 0)
#define BARRIER() asm volatile("s_waitcnt lgkmcnt(0)\n\ts_barrier" ::: "memory")
#define WAITVM(n) asm volatile("s_waitcnt vmcnt(" #n ")" ::: "memory")
#define WAITLDS() asm volatile("s_waitcnt lgkmcnt(0)" ::: "memory")
#define SC1 16

constexpr int PDELAY = 2, QDELAY = 1, QDELAYS = 9, PSLEEP = 6, QSLEEP = 16, QSLEEP2 = 8, OSLEEP = 2;

constexpr int S = 2048, D = 576;
constexpr int TILE_REC = 2304;
constexpr int HEAD_REC = 64 * TILE_REC;
constexpr int QTILE_REC = 1024;
constexpr float QSCALE = 0.23570226039551584f * 1.4426950408889634f;
constexpr unsigned SPIN_MAX = 1u << 16;
constexpr int CODE_LINES = 33;
constexpr unsigned WS_E = 0, WS_FP0 = 256, WS_FEND = WS_FP0 + 64 * 128, WS_GR = WS_FEND + 256 * 128, WS_GATE = WS_GR + 3 * 4 * 64 * 8,
                   WS_Q = WS_GATE + S * 4, WS_EXT = WS_Q + 32 * 64 * QTILE_REC, WS_KV = WS_EXT + 2 * 8 * 64 * 512, WS_KVT = WS_KV + 32 * HEAD_REC, WS_Z = WS_KVT + D * S * 2,
                   WS_O = WS_Z + D * S * 2;
constexpr int SLOT = 34816;
constexpr int HCOPY = 4160, HREC = 8 * HCOPY, UREC = 4608;
constexpr int L_H_A = SLOT, L_U_A = 4 * SLOT, L_X_A = 0;
constexpr int L_PK = 0, L_PV = 38912, L_PO = L_PV + 36864, L_SH = L_PO + 36864, L_GX = L_SH + 3072, L_GW = L_GX + 8 * 5120;
constexpr int L_H_B2 = 111616, L_U_B = L_H_B2 + HREC, L_X_B = L_PO;
constexpr int L_WO = 0, L_RED = 82944, L_RED2 = L_RED + 49152, L_Z = L_RED2 + 16384;
constexpr int L_MX = 158720, L_SYNC = L_MX + 4096;
enum { SY_SLOT0 = 0, SY_READY = 1, SY_XCH = 5, SY_CONVDONE = 9, SY_P0 = 10, SY_QKV = 11, SY_P3 = 12, SY_QKV1 = 13, SY_ZD = 14, SY_ZS = 15, SY_X3 = 16, SY_NM = 20, SY_PUB = 21 };
constexpr unsigned POISON = 0xffffffffu;

DI unsigned cvtpk(float a, float b) { f32x2 v = {a, b}; bf2 r = __builtin_convertvector(v, bf2); return __builtin_bit_cast(unsigned, r); }
DI i32x4 mkrsrc(const void* p, unsigned bytes) { i32x4 r; r[0] = __builtin_amdgcn_readfirstlane((int)(unsigned)(size_t)p); r[1] = __builtin_amdgcn_readfirstlane((int)(((size_t)p) >> 32) & 0xffff); r[2] = (int)bytes; r[3] = 0x00020000; return r; }
DI void dma16(i32x4 rsrc, unsigned voff, unsigned soff, unsigned ldsaddr) {
    asm volatile("s_mov_b32 m0, %3\n\ts_nop 0\n\tbuffer_load_dwordx4 %0, %1, %2 offen lds" :: "v"(voff), "s"(rsrc), "s"(soff), "s"(ldsaddr) : "memory");
}
DI void dma16x4(i32x4 rsrc, unsigned v0, unsigned v1, unsigned v2, unsigned v3, unsigned soff, unsigned ldsaddr) {
    asm volatile("s_mov_b32 m0, %6\n\ts_nop 0\n\tbuffer_load_dwordx4 %0, %4, %5 offen lds\n\tbuffer_load_dwordx4 %1, %4, %5 offen offset:1024 lds\n\t"
                 "buffer_load_dwordx4 %2, %4, %5 offen offset:2048 lds\n\tbuffer_load_dwordx4 %3, %4, %5 offen offset:3072 lds"
                 :: "v"(v0), "v"(v1), "v"(v2), "v"(v3), "s"(rsrc), "s"(soff), "s"(ldsaddr) : "memory");
}
DI void touch(i32x4 rsrc, unsigned voff, unsigned ldsaddr) {
    asm volatile("s_mov_b32 m0, %2\n\ts_nop 0\n\tbuffer_load_dword %0, %1, 0 offen lds" :: "v"(voff), "s"(rsrc), "s"(ldsaddr) : "memory");
}
DI s16x4 trread(ldsp p) { return __builtin_bit_cast(s16x4, __builtin_amdgcn_ds_read_tr16_b64_v4i16((LAS v4i16_t*)p)); }
DI float bflo(unsigned u) { return __uint_as_float(u << 16); }
DI float bfhi(unsigned u) { return __uint_as_float(u & 0xffff0000u); }
DI float dot2(unsigned a, unsigned b, float c) { return __builtin_amdgcn_fdot2_f32_bf16(__builtin_bit_cast(bf2, a), __builtin_bit_cast(bf2, b), c, false); }
DI float dot2sq(unsigned u, float c) { const bf2 v = __builtin_bit_cast(bf2, u); return __builtin_amdgcn_fdot2_f32_bf16(v, v, c, false); }
DI unsigned wave_umax(unsigned v) {
    v = max(v, (unsigned)__builtin_amdgcn_update_dpp(0, (int)v, 0x111, 0xf, 0xf, false));
    v = max(v, (unsigned)__builtin_amdgcn_update_dpp(0, (int)v, 0x112, 0xf, 0xf, false));
    v = max(v, (unsigned)__builtin_amdgcn_update_dpp(0, (int)v, 0x114, 0xf, 0xf, false));
    v = max(v, (unsigned)__builtin_amdgcn_update_dpp(0, (int)v, 0x118, 0xf, 0xf, false));
    v = max(v, (unsigned)__builtin_amdgcn_update_dpp(0, (int)v, 0x142, 0xa, 0xf, false));
    v = max(v, (unsigned)__builtin_amdgcn_update_dpp(0, (int)v, 0x143, 0xc, 0xf, false));
    return (unsigned)__builtin_amdgcn_readlane((int)v, 63);
}
DI unsigned ld_flag(const char* p) { return __hip_atomic_load((const GAS unsigned*)p, __ATOMIC_RELAXED, __HIP_MEMORY_SCOPE_AGENT); }
DI unsigned long long ld_gran(const char* p) { return __hip_atomic_load((const GAS unsigned long long*)p, __ATOMIC_RELAXED, __HIP_MEMORY_SCOPE_AGENT); }
DI void st_flag(char* p, unsigned v) { __hip_atomic_store((GAS unsigned*)p, v, __ATOMIC_RELAXED, __HIP_MEMORY_SCOPE_AGENT); }
DI void st_gran(char* p, unsigned long long v) { __hip_atomic_store((GAS unsigned long long*)p, v, __ATOMIC_RELAXED, __HIP_MEMORY_SCOPE_AGENT); }
DI void poll_flags(const char* p, bool on, unsigned tag) {
#pragma unroll 1
    for (unsigned spin = 0; spin < SPIN_MAX; ++spin) {
        const unsigned v = on ? ld_flag(p) : tag;
        if (__all(v == tag)) break;
        __builtin_amdgcn_s_sleep(PSLEEP);
    }
    FENCE();
}
DI void lds_inc(ldsp lds, int w) {
    unsigned long long sv;
    asm volatile("s_mov_b64 %0, exec\n\ts_mov_b64 exec, 1\n\tds_add_u32 %1, %2\n\ts_mov_b64 exec, %0" : "=&s"(sv) : "v"((unsigned)(size_t)lds + L_SYNC + 4 * w), "v"(1u) : "memory");
}
DI unsigned lds_inc_ret(ldsp lds, int w) {
    unsigned long long sv; unsigned old;
    asm volatile("s_mov_b64 %0, exec\n\ts_mov_b64 exec, 1\n\tds_add_rtn_u32 %1, %2, %3\n\ts_waitcnt lgkmcnt(0)\n\ts_mov_b64 exec, %0" : "=&s"(sv), "=&v"(old) : "v"((unsigned)(size_t)lds + L_SYNC + 4 * w), "v"(1u) : "memory");
    return (unsigned)__builtin_amdgcn_readfirstlane((int)old);
}
DI unsigned lds_wait(ldsp lds, int w, unsigned n) {
    unsigned v = 0;
#pragma unroll 1
    for (unsigned spin = 0; spin < SPIN_MAX * 64; ++spin) {
        v = __builtin_amdgcn_readfirstlane(__hip_atomic_load((LAS unsigned*)(lds + L_SYNC + 4 * w), __ATOMIC_RELAXED, __HIP_MEMORY_SCOPE_WORKGROUP));
        if (v >= n) break;
    }
    FENCE();
    return v;
}
DI void lds_set(ldsp lds, int w, unsigned v) { FENCE(); __hip_atomic_store((LAS unsigned*)(lds + L_SYNC + 4 * w), v, __ATOMIC_RELAXED, __HIP_MEMORY_SCOPE_WORKGROUP); FENCE(); }

struct Args { const char *x, *wqkv, *wo, *bk, *bv, *shk, *ssmk, *ssmd, *gw; char *out, *ws; };


struct HTaps { u32x4 a[2], b[2]; };
DI void conv_h_load(const Args& a, int ch, int hf, int lane, HTaps& h) {
    const auto rh = __builtin_amdgcn_make_buffer_rsrc((void*)(a.ssmk + ch * 4096), 0, 4096, 0x00020000);
#pragma unroll
    for (int it = 0; it < 2; ++it) { const int p = 128 * hf + 64 * it + lane - 1;
        h.a[it] = __builtin_amdgcn_raw_buffer_load_b128(rh, p < 0 ? 0x80000000u : p * 16, 0, 0);
        h.b[it] = __builtin_amdgcn_raw_buffer_load_b128(rh, p * 16 + 16, 0, 0); }
}
DI void conv_h_store(ldsp lh, int hf, int lane, HTaps h) {
    PIN(h.a[0]); PIN(h.b[0]); PIN(h.a[1]); PIN(h.b[1]);
#pragma unroll
    for (int it = 0; it < 2; ++it) {
        const unsigned d[8] = {h.a[it][0], h.a[it][1], h.a[it][2], h.a[it][3], h.b[it][0], h.b[it][1], h.b[it][2], h.b[it][3]};
        unsigned e[7];
#pragma unroll
        for (int k = 0; k < 7; ++k) e[k] = __builtin_amdgcn_alignbit(d[k + 1], d[k], 16);
        const ldsp p = lh + (128 * hf + 64 * it + lane + 1) * 16;
#pragma unroll
        for (int c = 0; c < 4; ++c) {
            *(LAS u32x4*)(p + (2 * c) * HCOPY) = (u32x4){d[c], d[c + 1], d[c + 2], d[c + 3]};
            *(LAS u32x4*)(p + (2 * c + 1) * HCOPY) = (u32x4){e[c], e[c + 1], e[c + 2], e[c + 3]};
        }
    }
    if (hf == 0) { if (lane < 8) *(LAS u32x4*)(lh + lane * HCOPY) = (u32x4){0u, 0u, 0u, 0u}; }
    else if (lane == 63) *(LAS u32x4*)(lh + 257 * 16) = h.b[1];
}

struct ConvMid { f32x4 own[4]; f32x2 dco; };
DI void conv_products(const Args& a, ldsp lds, int ch, int hf, int pair, int lh, int lu, int lx, unsigned nslot0, const HTaps& ht, int lane, ConvMid& cm) {
    const int l15 = lane & 15, kq = lane >> 4;
    lds_wait(lds, SY_P0, 1);
    const auto rkt = __builtin_amdgcn_make_buffer_rsrc((void*)(a.ws + WS_KVT), 0, D * S * 2, 0x00020000);
    const u32x4 u0 = __builtin_amdgcn_raw_buffer_load_b128(rkt, lane * 16, ch * 4096 + hf * 2048, SC1);
    const u32x4 u1 = __builtin_amdgcn_raw_buffer_load_b128(rkt, lane * 16, ch * 4096 + hf * 2048 + 1024, SC1);
    const float dco = bflo(*(const GAS unsigned short*)(a.ssmd + ch * 2));
    lds_wait(lds, SY_SLOT0, nslot0);
    conv_h_store(lds + lh, hf, lane, ht);
    *(LAS u32x4*)(lds + lu + hf * 2048 + lane * 16) = u0;
    *(LAS u32x4*)(lds + lu + hf * 2048 + 1024 + lane * 16) = u1;
    if (hf == 0) { if (lane < 32) *(LAS u32x4*)(lds + lu + 4096 + lane * 16) = (u32x4){0u, 0u, 0u, 0u}; }
    WAITLDS();
    lds_inc(lds, SY_READY + pair);
    unsigned pao = lh + ((l15 + 1) & 7) * HCOPY + (((l15 + 1) >> 3) + kq) * 16 + hf * 256, pbo = lu + (4064 - 3584) - 32 * l15 + 16 * kq + hf * 256;
    PIN(pao); PIN(pbo);
    lds_wait(lds, SY_READY + pair, 2);
    const ldsp pa = lds + pao, pb = lds + pbo;
    f32x4 acc[8];
#pragma unroll
    for (int c = 0; c < 8; ++c) acc[c] = (f32x4){0.f, 0.f, 0.f, 0.f};
    bf16x8 A0[8], B0[8], A1[8], B1[8];
#define CRD(A, B, k0, i) do { A[i] = *(const LAS bf16x8*)(pa + (k0) * 64 + (i) * 512); B[i] = *(const LAS bf16x8*)(pb + (k0) * 64 + (7 - (i)) * 512); } while (0)
#define CMM(A, B, m, e) do { acc[(m) + (e)] = MFMAS(A[m], B[e], acc[(m) + (e)]); } while (0)
#define CLASS(A, B, NA, NB, k1, pre) do { \
        _Pragma("unroll") for (int m = 0; m < 8; ++m) { \
            if (pre) { CRD(NA, NB, k1, m); } SBAR(); \
            _Pragma("unroll") for (int e = 0; e < 8 - m; ++e) CMM(A, B, m, e); \
            SBAR(); \
        } } while (0)
#pragma unroll
    for (int i = 0; i < 8; ++i) CRD(A0, B0, 0, i);
    SBAR();
    CLASS(A0, B0, A1, B1, 1, true);
    CLASS(A1, B1, A0, B0, 2, true);
    CLASS(A0, B0, A1, B1, 3, true);
    CLASS(A1, B1, A0, B0, 0, false);
#pragma unroll
    for (int i = 0; i < 4; ++i) { const f32x4 give = hf ? acc[i] : acc[4 + i]; cm.own[i] = hf ? acc[4 + i] : acc[i];
        *(LAS f32x4*)(lds + lx + hf * 4096 + i * 1024 + lane * 16) = give; }
    lds_inc(lds, SY_XCH + pair);
    cm.dco = (f32x2){dco, dco}; PIN(cm.dco);
}
DI void conv_finish(ldsp lds, int hf, int pair, int lu, int lx, int lane, const ConvMid& cm, u32x4 (&zp)[2]) {
    const int l15 = lane & 15, kq = lane >> 4;
    lds_wait(lds, SY_XCH + pair, 2);
    unsigned zw[4][2];
#pragma unroll
    for (int i = 0; i < 4; ++i) {
        const f32x4 y = cm.own[i] + *(const LAS f32x4*)(lds + lx + (hf ^ 1) * 4096 + i * 1024 + lane * 16);
        const int t = 256 * (4 * hf + i) + 16 * l15 + 4 * kq;
        const u32x2 u = *(const LAS u32x2*)(lds + lu + 2 * (2044 - t));
        const f32x2 y0 = (f32x2){y[0], y[1]} + (f32x2){bfhi(u[1]), bflo(u[1])} * cm.dco, y1 = (f32x2){y[2], y[3]} + (f32x2){bfhi(u[0]), bflo(u[0])} * cm.dco;
        zw[i][0] = cvtpk(y0[0], y0[1]); zw[i][1] = cvtpk(y1[0], y1[1]);
    }
#pragma unroll
    for (int i = 0; i < 4; i += 2) {
        const auto s0 = __builtin_amdgcn_permlane16_swap(zw[i][0], zw[i + 1][0], false, false), s1 = __builtin_amdgcn_permlane16_swap(zw[i][1], zw[i + 1][1], false, false);
        zp[i >> 1] = (u32x4){s0[0], s1[0], s0[1], s1[1]};
    }
    WAITLDS();
    lds_inc(lds, SY_CONVDONE);
}

extern "C" __global__ __launch_bounds__(512, 2) void gated_mix_fused(Args a) {
    extern __shared__ __attribute__((aligned(16))) char smem[];
    const ldsp lds = (ldsp)smem; const unsigned lds0 = (unsigned)(size_t)smem;
    const int tid = threadIdx.x, lane = tid & 63;
    const int wid = __builtin_amdgcn_readfirstlane(tid >> 6);
    const int b = blockIdx.x, xcd = b & 7, r = b >> 3;
    unsigned long long pc0; asm volatile("s_getpc_b64 %0" : "=s"(pc0));
    unsigned e_raw;
#define ARGS() do { asm volatile("" :: "s"(a.x), "s"(a.wqkv), "s"(a.wo), "s"(a.bk), "s"(a.bv), "s"(a.shk), "s"(a.ssmk), "s"(a.ssmd), "s"(a.gw), "s"(a.out), "s"(a.ws)); \
        e_raw = *(const __attribute__((address_space(4))) unsigned*)(a.ws + WS_E); FENCE(); } while (0)
    if (tid < 32) *(LAS unsigned*)(lds + L_SYNC + 4 * tid) = 0u;
    auto get_tag = [&]() { unsigned t = __builtin_amdgcn_readfirstlane(e_raw); asm volatile("" : "+s"(t)); return t + 1u; };
    const int hp = 4 * (xcd & 3) + (r & 3), j = 8 * (xcd >> 2) + (r >> 2);
    int nconv, cv_ch = 0, cv_lh = 0, cv_lu = 0, cv_lx = 0, cv_pair = 0;
    bool cv_on = false;
    HTaps ht;
    if (__builtin_expect(r < 24, 1)) {
        const int jb = 8 * (xcd >> 2) + r / 3, mat = r % 3, hg = xcd & 3;
        nconv = 4;
        if (__builtin_expect(wid >= 4, 1)) {
            const int pw = wid - 4;
            const int pair = pw >> 1, hf = pw & 1, ch = 72 * xcd + 2 * r + pair;
            const int ko = 3 * (jb % 3);
            if (pw < 2) {
                unsigned vx[8];
#pragma unroll
                for (int n = 0; n < 8; ++n) { vx[n] = (8 * (8 * pw + n) + (lane >> 3)) * 1152 + (((lane & 7) ^ ((4 * (n & 1) + (lane >> 4)) & 7)) * 16) - (n & 3) * 1024; PIN(vx[n]); }
                const char* px = a.x; asm volatile("" : "+s"(px));
                const i32x4 rx = mkrsrc(px, S * D * 2);
                const unsigned sx = 128 * jb * 1152, dst = lds0 + pw * 8192;
                auto chunk = [&](int c) {
                    const int cc = c + ko >= 9 ? c + ko - 9 : c + ko;
                    const unsigned slot = dst + (c & 3) * SLOT, so = sx + cc * 128;
                    dma16x4(rx, vx[0], vx[1], vx[2], vx[3], so, slot); dma16x4(rx, vx[4], vx[5], vx[6], vx[7], so, slot + 4096);
                };
                chunk(0); chunk(1);
                ARGS();
                WAITVM(8); BARRIER();
                chunk(2); chunk(3);
#pragma unroll 1
                for (int c = 1; c < 6; ++c) { WAITVM(16); BARRIER(); chunk(c + 3); }
                WAITVM(16); BARRIER();
                WAITVM(8); BARRIER();
                conv_h_load(a, ch, hf, lane, ht);
            } else {
                unsigned vw[9];
#pragma unroll
                for (int n = 0; n < 9; ++n) { const int q = 64 * (9 * hf + n) + lane, rho = (q * 3641) >> 16, pc = q - 18 * rho;
                    const int k = (rho & 32) + ((rho >> 4) & 1) * 16 + ((rho >> 2) & 1) * 8 + ((rho >> 3) & 1) * 4 + (rho & 3);
                    vw[n] = k * 3456 + pc * 16 - (n < 8 ? (n & 3) * 1024 : 0); PIN(vw[n]); }
                const char* pq = a.wqkv; asm volatile("" : "+s"(pq));
                const i32x4 rw = mkrsrc(pq, D * 3 * D * 2);
                const unsigned sw = mat * 1152 + 288 * hg, dst = lds0 + 16384 + hf * 9216;
                auto chunk = [&](int c) {
                    const int cc = c + ko >= 9 ? c + ko - 9 : c + ko;
                    const unsigned slot = dst + (c & 3) * SLOT, so = sw + cc * (64 * 3456);
                    dma16x4(rw, vw[0], vw[1], vw[2], vw[3], so, slot); dma16x4(rw, vw[4], vw[5], vw[6], vw[7], so, slot + 4096); dma16(rw, vw[8], so, slot + 8192);
                };
                chunk(0); chunk(1);
                ARGS();
                WAITVM(9); BARRIER();
                chunk(2); chunk(3);
#pragma unroll 1
                for (int c = 1; c < 6; ++c) { WAITVM(18); BARRIER(); chunk(c + 3); }
                WAITVM(18); BARRIER();
                WAITVM(9); BARRIER();
                conv_h_load(a, ch, hf, lane, ht);
            }
            WAITVM(4); BARRIER();
            cv_on = true; cv_ch = ch; cv_pair = pair; cv_lh = L_H_A + pair * HREC; cv_lu = L_U_A + pair * UREC; cv_lx = L_X_A + pair * 8192;
        } else {
            ARGS();
            const int l15 = lane & 15, kq = lane >> 4, q4 = l15 >> 2, p4 = lane & 3;
            unsigned ax[2];
#pragma unroll
            for (int s = 0; s < 2; ++s) ax[s] = (32 * wid + l15) * 128 + (((4 * s + kq) ^ (l15 >> 1)) * 16);
            const unsigned aw = 16384 + (16 * (kq >> 1) + 4 * (kq & 1) + q4) * 288 + p4 * 8, awe = aw + p4 * 64 + 32;
            f32x4 acc[2][9];
#pragma unroll
            for (int nt = 0; nt < 2; ++nt)
#pragma unroll
                for (int m = 0; m < 9; ++m) acc[nt][m] = (f32x4){0.f, 0.f, 0.f, 0.f};
            struct Fr { s16x4 lo[9], hi[9]; bf16x8 x0, x1; };
            Fr F0, F1;
#define RD_X(N, slot, s) do { N.x0 = *(const LAS bf16x8*)(slot + ax[s]); N.x1 = *(const LAS bf16x8*)(slot + ax[s] + 2048); } while (0)
#define WOFF(m) ((m) < 8 ? aw + 72 * ((m) >> 1) + 40 * ((m) & 1) : awe)
#define RD_W(N, slot, s, m) do { N.lo[m] = trread(slot + WOFF(m) + (s) * 9216); N.hi[m] = trread(slot + WOFF(m) + (s) * 9216 + 2304); } while (0)
#define MM(C, m) do { const bf16x8 w_ = __builtin_shufflevector(C.lo[m], C.hi[m], 0, 1, 2, 3, 4, 5, 6, 7); \
                if constexpr (VMAT) { acc[0][m] = MFMAS(C.x0, w_, acc[0][m]); acc[1][m] = MFMAS(C.x1, w_, acc[1][m]); } \
                else { acc[0][m] = MFMAS(w_, C.x0, acc[0][m]); acc[1][m] = MFMAS(w_, C.x1, acc[1][m]); } PIN(acc[0][m]); PIN(acc[1][m]); } while (0)
#define HALF(C, N, slot, s) do { \
                RD_X(N, slot, s); RD_W(N, slot, s, 0); SBAR(); MM(C, 0); SBAR(); \
                RD_W(N, slot, s, 1); SBAR(); MM(C, 1); SBAR(); RD_W(N, slot, s, 2); SBAR(); MM(C, 2); SBAR(); \
                RD_W(N, slot, s, 3); SBAR(); MM(C, 3); SBAR(); RD_W(N, slot, s, 4); SBAR(); MM(C, 4); SBAR(); \
                RD_W(N, slot, s, 5); SBAR(); MM(C, 5); SBAR(); RD_W(N, slot, s, 6); SBAR(); MM(C, 6); SBAR(); \
                RD_W(N, slot, s, 7); SBAR(); MM(C, 7); SBAR(); RD_W(N, slot, s, 8); SBAR(); MM(C, 8); SBAR(); } while (0)
            const int tile = 4 * jb + wid;
            const unsigned hs = mat == 0 ? 64 * QTILE_REC : HEAD_REC;
            const char* rec = a.ws + (mat == 0 ? WS_Q + (size_t)(8 * hg) * (64 * QTILE_REC) + tile * QTILE_REC : WS_KV + (size_t)(8 * hg) * HEAD_REC + tile * TILE_REC + (mat - 1) * 1024);
            const char* rext = a.ws + WS_EXT + (((mat & 1) * 8 + 2 * hg) * 64 + tile) * 512;
            asm volatile("" : "+s"(rec), "+s"(rext));
            auto gemm = [&](auto vm) {
                constexpr bool VMAT = decltype(vm)::value;
                {
                    BARRIER();
                    const ldsp slot = lds;
                    RD_X(F0, slot, 0);
#pragma unroll
                    for (int m = 0; m < 9; ++m) RD_W(F0, slot, 0, m);
                    SBAR();
                    HALF(F0, F1, slot, 1);
                }
#pragma unroll 1
                for (int c = 1; c < 9; ++c) {
                    BARRIER();
                    const ldsp slot = lds + (c & 3) * SLOT;
                    HALF(F1, F0, slot, 0);
                    HALF(F0, F1, slot, 1);
                }
                WAITLDS();
                lds_inc(lds, SY_SLOT0);
#pragma unroll
                for (int m = 0; m < 9; ++m) MM(F1, m);
            };
            if (mat == 2) gemm(std::true_type{}); else gemm(std::false_type{});
            const auto rr = __builtin_amdgcn_make_buffer_rsrc((void*)rec, 0, 8 * HEAD_REC, 0x00020000);
            u32x4 pc[9];
            if (mat < 2) {
                const float qs = mat == 0 ? QSCALE : 1.f;
#pragma unroll
                for (int m = 0; m < 9; ++m) {
                    acc[0][m] *= qs; acc[1][m] *= qs;
                    const auto s0 = __builtin_amdgcn_permlane16_swap(cvtpk(acc[0][m][0], acc[0][m][1]), cvtpk(acc[1][m][0], acc[1][m][1]), false, false);
                    const auto s1 = __builtin_amdgcn_permlane16_swap(cvtpk(acc[0][m][2], acc[0][m][3]), cvtpk(acc[1][m][2], acc[1][m][3]), false, false);
                    if (m < 8) { pc[m] = (u32x4){s0[0], s1[0], s0[1], s1[1]}; __builtin_amdgcn_raw_buffer_store_b128(pc[m], rr, lane * 16, m * hs, SC1); }
                    else {
                        pc[m] = (u32x4){s0[0], s1[0], s0[1], s1[1]};
                        const auto re = __builtin_amdgcn_make_buffer_rsrc((void*)rext, 0, 2 * 64 * 512, 0x00020000);
                        __builtin_amdgcn_raw_buffer_store_b128(pc[m], re, (lane >> 5) * (64 * 512) + (lane & 31) * 16, 0, SC1);
                    }
                    SBAR();
                }
            } else {
#pragma unroll
                for (int m = 0; m < 9; ++m) {
                    const auto s0 = __builtin_amdgcn_permlane32_swap(cvtpk(acc[0][m][0], acc[0][m][1]), cvtpk(acc[1][m][0], acc[1][m][1]), false, false);
                    const auto s1 = __builtin_amdgcn_permlane32_swap(cvtpk(acc[0][m][2], acc[0][m][3]), cvtpk(acc[1][m][2], acc[1][m][3]), false, false);
                    pc[m] = (u32x4){s0[0], s1[0], s0[1], s1[1]};
                    if (m < 8) __builtin_amdgcn_raw_buffer_store_b128(pc[m], rr, lane * 16, m * hs, SC1);
                    else __builtin_amdgcn_raw_buffer_store_b128(pc[m], rr, (l15 >> 1) * hs + 1024 + kq * 32 + (l15 & 1) * 16, 0, SC1);
                    SBAR();
                }
                __builtin_amdgcn_raw_buffer_store_b128((u32x4){0x3f803f80u, 0x3f803f80u, 0x3f803f80u, 0x3f803f80u}, rr, (lane >> 3) * hs + 1152 + (lane & 7) * 16, 0, SC1);
            }
            SBAR();
            unsigned nm = 0u;
#pragma unroll
            for (int m = 0; m < 9; ++m) nm = max(nm, __float_as_uint(dot2sq(pc[m][0], dot2sq(pc[m][1], dot2sq(pc[m][2], dot2sq(pc[m][3], 0.f))))));
            nm = wave_umax(nm);
            {
                unsigned long long sv; unsigned old, all;
                asm volatile("s_mov_b64 %0, exec\n\ts_mov_b64 exec, 1\n\tds_max_u32 %1, %2 offset:%3\n\ts_mov_b64 exec, %0" : "=&s"(sv) : "v"(lds0 + L_SYNC), "v"(nm), "n"(4 * SY_NM) : "memory");
                char* gp_ = a.ws + WS_GR + ((hg * 3 + mat) * 16 + jb) * 8;
                const unsigned tg = get_tag();
                asm volatile("" : "+s"(gp_));
                WAITVM(0);
                asm volatile("s_mov_b64 %0, exec\n\ts_mov_b64 exec, 1\n\tds_add_rtn_u32 %1, %3, %4 offset:%6\n\tds_read_b32 %2, %3 offset:%5\n\ts_waitcnt lgkmcnt(0)\n\ts_mov_b64 exec, %0"
                             : "=&s"(sv), "=&v"(old), "=&v"(all) : "v"(lds0 + L_SYNC), "v"(1u), "n"(4 * SY_NM), "n"(4 * SY_PUB) : "memory");
                if (__builtin_amdgcn_readfirstlane((int)old) == 3) {
                    if (lane == 0) st_gran(gp_, ((unsigned long long)tg << 32) | all);
                }
            }
        }
    } else {
        const int dg = r - 24, tt = 4 * xcd + (dg >> 1), c2 = dg & 1;
        nconv = 6;
        if (wid == 7) touch(mkrsrc((const void*)(pc0 & ~127ull), 8 * CODE_LINES * 128), lane < CODE_LINES ? (dg * CODE_LINES + lane) * 128 : 0x80000000u, lds0 + L_MX);
        const int q = 64 * wid + lane, row = (q * 1821) >> 16, q8 = 512 + lane, row8 = (q8 * 1821) >> 16;
        unsigned va = row * 1152 + (q - 36 * row) * 16, vb = row8 * 1152 + (q8 - 36 * row8) * 16, va0 = (tt == 0 && row < 3) ? 0x80000000u : va;
        PIN(va); PIN(vb); PIN(va0);
        const char *pk = a.bk, *pv = a.bv; asm volatile("" : "+s"(pk), "+s"(pv));
        auto xrows = [&]() {
            const i32x4 rx = mkrsrc(a.x + (256 * xcd + 32 * dg + 4 * wid) * 1152, 4608);
            const unsigned v16 = lane * 16;
            dma16x4(rx, v16, v16, v16, v16, 0, lds0 + L_GX + wid * 5120); dma16(rx, v16 + 4096, 0, lds0 + L_GX + wid * 5120 + 4096);
        };
        {
            constexpr int PSTEP = 16 * 1152;
            const i32x4 rk = mkrsrc(pk + (64 * tt - 3) * 1152 + c2 * 576, 66 * 1152 + 576), rv = mkrsrc(pv + 64 * tt * 1152 + c2 * 576, 63 * 1152 + 576);
            dma16(rk, va0, 0, lds0 + L_PK + wid * 1024);
            dma16(rv, va, 0, lds0 + L_PV + wid * 1024);
#pragma unroll
            for (int p = 1; p < 4; ++p) { dma16(rk, va, p * PSTEP, lds0 + L_PK + (9 * p + wid) * 1024); dma16(rv, va, p * PSTEP, lds0 + L_PV + (9 * p + wid) * 1024); }
            if (wid < 4) dma16(rk, vb, wid * PSTEP, lds0 + L_PK + (9 * wid + 8) * 1024); else dma16(rv, vb, (wid - 4) * PSTEP, lds0 + L_PV + (9 * (wid - 4) + 8) * 1024);
            if (wid < 2) dma16(rk, q < 108 ? va : 0x80000000u, 4 * PSTEP, lds0 + L_PK + (36 + wid) * 1024);
            if (wid >= 5) dma16(mkrsrc(a.shk + c2 * 2304, 2304), lane * 16 + (wid - 5) * 1024, 0, lds0 + L_SH + (wid - 5) * 1024);
            if (wid >= 2 && wid < 4) dma16(mkrsrc(a.gw, 1152), lane * 16 + (wid - 2) * 1024, 0, lds0 + L_GW + (wid - 2) * 1024);
            ARGS();
            xrows();
        }
        const int ob = (tid * 456) >> 16;
        int cp = tid - 144 * ob, o0 = ob < 3 ? ob : 6, o1 = ob < 3 ? ob + 3 : 7;
        unsigned kvo = (tid >> 3) * 4096 + (tid & 7) * 16, kvs = L_PO + (tid >> 3) * 128 + (((tid & 7) ^ ((tid >> 4) & 7)) * 16);
        const char* pkvt = a.ws + WS_KVT + 288 * c2 * 4096 + (1984 - 64 * tt) * 2;
        char* pfl = a.ws + WS_FP0 + (32 * c2 + tt) * 128;
        PIN(cp); PIN(o0); PIN(o1); PIN(kvo); PIN(kvs); asm volatile("" : "+s"(pkvt), "+s"(pfl));
        WAITVM(5);
        BARRIER();
        const int pair = wid >> 1, hf = wid & 1, ch = 72 * xcd + 48 + 3 * dg + pair;
        if (wid < 6) { conv_h_load(a, ch, hf, lane, ht); FENCE(); }
        {
            const bool third = (wid & 6) == 2;
            f32x2 s0, s1, s2, s3;
            auto taps = [&](const u32x4 sh) { s0 = (f32x2){bflo(sh[0]), bflo(sh[2])}; s1 = (f32x2){bfhi(sh[0]), bfhi(sh[2])}; s2 = (f32x2){bflo(sh[1]), bflo(sh[3])}; s3 = (f32x2){bfhi(sh[1]), bfhi(sh[3])}; };
            unsigned ku[3][11], vu[3][8];
            auto rd1 = [&](int n, int o, int c, int i) {
                const ldsp kp = lds + L_PK + o * 4608 + c * 4;
                if (i < 11) ku[n][i] = *(const LAS unsigned*)(kp + i * 576); else vu[n][i - 11] = *(const LAS unsigned*)(kp + (L_PV - L_PK) + (i - 11) * 576);
                FENCE();
            };
            auto cmp = [&](int n, int o, int c, bool next, int on, int cn) {
                f32x2 kk[11], kv[8];
#pragma unroll
                for (int i = 0; i < 11; ++i) { if (next) rd1(n + 1, on, cn, i); kk[i] = (f32x2){bflo(ku[n][i]), bfhi(ku[n][i])}; PIN(kk[i]); SBAR(); }
#pragma unroll
                for (int e = 0; e < 8; ++e) { if (next) rd1(n + 1, on, cn, 11 + e);
                    const f32x2 vv = {bflo(vu[n][e]), bfhi(vu[n][e])};
                    kv[e] = (kk[e] * s3 + kk[e + 1] * s2 + kk[e + 2] * s1 + kk[e + 3] * s0) * vv; PIN(kv[e]); }
                u32x4 w0, w1;
#pragma unroll
                for (int i = 0; i < 4; ++i) { w0[i] = cvtpk(kv[7 - 2 * i][0], kv[6 - 2 * i][0]); w1[i] = cvtpk(kv[7 - 2 * i][1], kv[6 - 2 * i][1]); }
                const int pos = (7 - o) ^ (c & 7);
                *(LAS u32x4*)(lds + L_PO + c * 256 + pos * 16) = w0;
                *(LAS u32x4*)(lds + L_PO + c * 256 + 128 + pos * 16) = w1;
            };
            const int o2 = third ? 4 + wid : 0, c3 = 80 + lane;
            const u32x4 sha = *(const LAS u32x4*)(lds + L_SH + cp * 16);
            FENCE();
#pragma unroll
            for (int i = 0; i < 11; ++i) rd1(0, o0, cp, i);
            taps(sha); PIN(s0); PIN(s1); PIN(s2); PIN(s3);
#pragma unroll
            for (int i = 11; i < 19; ++i) rd1(0, o0, cp, i);
            SBAR();
            cmp(0, o0, cp, true, o1, cp);
            const u32x4 shb = *(const LAS u32x4*)(lds + L_SH + c3 * 16);
            cmp(1, o1, cp, true, o2, c3);
            if (third) { taps(shb); cmp(2, o2, c3, false, 0, 0); }
        }
        BARRIER();
        const int lh = pair < 2 ? pair * HREC : L_H_B2;
        {
            const auto rkvt = __builtin_amdgcn_make_buffer_rsrc((void*)pkvt, 0, 288 * 4096, 0x00020000);
            const ldsp src = lds + kvs;
            u32x4 v[5];
#pragma unroll
            for (int n = 0; n < 5; ++n) if (n < 4 || wid < 4) v[n] = *(const LAS u32x4*)(src + n * 8192);
#pragma unroll
            for (int n = 0; n < 5; ++n) if (n < 4 || wid < 4) __builtin_amdgcn_raw_buffer_store_b128(v[n], rkvt, kvo, n * 262144, SC1);
            FENCE();
        }
        WAITVM(0);
        BARRIER();
        if (tid == 0) st_flag(pfl, get_tag());
        {
            const ldsp xr = lds + L_GX + wid * 5120 + (lane >> 4) * 1152 + (lane & 15) * 8, wr = lds + L_GW + (lane & 15) * 8;
            float s = 0.f;
#pragma unroll
            for (int i = 0; i < 9; ++i) { const u32x2 xv = *(const LAS u32x2*)(xr + i * 128), wv = *(const LAS u32x2*)(wr + i * 128);
                s = dot2(xv[0], wv[0], s); s = dot2(xv[1], wv[1], s); }
            s += __int_as_float(__builtin_amdgcn_update_dpp(0, __float_as_int(s), 0x111, 0xf, 0xf, true));
            s += __int_as_float(__builtin_amdgcn_update_dpp(0, __float_as_int(s), 0x112, 0xf, 0xf, true));
            s += __int_as_float(__builtin_amdgcn_update_dpp(0, __float_as_int(s), 0x114, 0xf, 0xf, true));
            s += __int_as_float(__builtin_amdgcn_update_dpp(0, __float_as_int(s), 0x118, 0xf, 0xf, true));
            if ((lane & 15) == 15) __hip_atomic_store((GAS float*)(a.ws + WS_GATE) + 256 * xcd + 32 * dg + 4 * wid + (lane >> 4), s, __ATOMIC_RELAXED, __HIP_MEMORY_SCOPE_AGENT);
            WAITLDS();
            lds_inc(lds, SY_SLOT0);
        }
        if (wid == 6) { poll_flags(a.ws + WS_FP0 + (32 * (xcd >> 2) + (lane & 31)) * 128, lane < 32, get_tag()); lds_set(lds, SY_P0, 1); }
        cv_on = pair < 3; cv_ch = ch; cv_pair = pair; cv_lh = lh; cv_lu = L_U_B + pair * UREC; cv_lx = L_X_B + pair * 8192;
    }
    const int hd = wid >> 2, head = 2 * hp + hd, kq = wid & 3;
    const int r32 = lane & 31, hi = lane >> 5;
    const int rh = 4 * xcd + (r >> 3), cg = r & 7;
    const int gs = 2 * rh + (lane & 1);
    unsigned p3o = 4 * (lane < 16 ? 256 + 16 * (rh >> 1) + lane : lane < 48 ? 32 * cg + lane - 16 : 32 * (gs >> 3) + 24 + (gs & 7));
    PIN(p3o);
    auto p3flag = [&]() { return a.ws + WS_FEND + p3o; };
    const auto rxx = __builtin_amdgcn_make_buffer_rsrc((void*)a.x, 0, S * D * 2, 0x00020000);
    unsigned xo = (64 * rh + 16 * kq + (lane & 15)) * 1152 + cg * 144 + (lane >> 4) * 8 + hd * 96;
    const auto ro = __builtin_amdgcn_make_buffer_rsrc((void*)(a.ws + WS_O), 0, D * S * 2, 0x00020000);
    const unsigned orow = (8 * j + 2 * kq + (r32 >> 4)) * 18432 + (r32 & 15) * 16;
    unsigned oslot = orow + hp * 1024 + (2 * hd + hi) * 256, oslot_e = hi == 0 ? orow + (16 + (hp >> 3)) * 1024 + ((hp >> 1) & 3) * 256 + (hp & 1) * 8 : 0x80000000u;
    auto wsrc = [](int q, bool ext) { const int row = (q * 7282) >> 16, sp = row >> 5, g = (row >> 3) & 3, qq = (row >> 1) & 3, f = row & 1;
        return (unsigned)((ext ? 36 * (8 * sp + 2 * g + f) + 16 + qq : 36 * sp + 20 * (g >> 1) + 4 * (g & 1) + 8 * f + qq) * 1152 + (q - 9 * row) * 16); };
    unsigned wva = wsrc(64 * wid + lane, false), wvb = wsrc(512 + lane, false), wve = wsrc(64 * wid + lane, true), wvf = wsrc(512 + lane, true);
    unsigned zo = 512 * ((lane >> 4) & 1) + 32 * (lane & 15) + 16 * (lane >> 5);
    unsigned fo = 4 * (lane ? 256 + 16 * j + hp : 32 * xcd + r);
    const char *pkv = a.ws + WS_KV + (size_t)head * HEAD_REC, *pq_ = a.ws + WS_Q + ((size_t)head * 64 + 4 * j) * QTILE_REC;
    const char *pqe = a.ws + WS_EXT + ((head >> 2) * 64 + 4 * j) * 512, *pke = a.ws + WS_EXT + (8 + (head >> 2)) * 64 * 512;
    unsigned offK = lane * 16, offE = r32 * 16 + (head & 3) * 4, offEq = hi ? 0x80000000u : offE;
    unsigned offV0 = r32 < 16 ? 1024 + hi * 256 + r32 * 16 : r32 < 18 ? 2048 + hi * 32 + (r32 - 16) * 16 : r32 == 18 ? 2176 + (r32 & 7) * 16 : 0x80000000u;
    unsigned offV1 = offV0 + (r32 < 16 ? 512 : r32 < 18 ? 64 : 0);
    asm volatile("" : "+s"(pkv), "+s"(pq_), "+s"(pqe), "+s"(pke)); PIN(offK); PIN(offE); PIN(offEq); PIN(offV0); PIN(offV1);
    PIN(xo); PIN(oslot); PIN(oslot_e); PIN(wva); PIN(wvb); PIN(wve); PIN(wvf); PIN(zo); PIN(fo);
    u32x4 zp[2] = {{0u, 0u, 0u, 0u}, {0u, 0u, 0u, 0u}};
    ConvMid cm;
    u32x2 xv[3];
#pragma unroll
    for (int n = 0; n < 3; ++n) xv[n] = __builtin_amdgcn_raw_buffer_load_b64(rxx, (n == 0 || hd == 0 || (n == 1 && (lane >> 4) < 2)) ? xo + n * 32 : 0x80000000u, 0, 0);
    FENCE();
    if (r < 24 && wid == 4) {
        for (int n = 0; n < PDELAY; ++n) __builtin_amdgcn_s_sleep(8);
        poll_flags(a.ws + WS_FP0 + (32 * (xcd >> 2) + (lane & 31)) * 128, lane < 32, get_tag()); lds_set(lds, SY_P0, 1);
    }
    if (cv_on) conv_products(a, lds, cv_ch, wid & 1, cv_pair, cv_lh, cv_lu, cv_lx, r < 24 ? 4u : 8u, ht, lane, cm);

    const unsigned tag = get_tag();
    unsigned fv = tag;
    f32x16 O[4];
    {
        const bool poller = wid == (r < 24 ? 0 : 7);
        const char* gp = a.ws + WS_GR + (xcd & 3) * 384 + (lane < 48 ? lane : lane - 16) * 8;
        const int gb = lane & 15;
        const bool need2 = lane >= 16 || gb == j;
        unsigned long long gv = 0;
        bool have2 = true;
        if (poller) {
            for (int n = 0; n < (r < 24 ? QDELAY : QDELAYS); ++n) __builtin_amdgcn_s_sleep(8);
            const bool need1 = lane < 16 ? gb == j : ((gb - j) & 15) < 2;
#pragma unroll 1
            for (unsigned spin = 0; spin < SPIN_MAX; ++spin) {
                gv = ld_gran(gp);
                if (__all(!need1 || (unsigned)(gv >> 32) == tag)) break;
                __builtin_amdgcn_s_sleep(QSLEEP);
            }
            lds_set(lds, SY_QKV1, 1u);
            have2 = __all(!need2 || (unsigned)(gv >> 32) == tag);
        }
        lds_wait(lds, SY_QKV1, 1);
        const auto rkv = __builtin_amdgcn_make_buffer_rsrc((void*)pkv, 0, HEAD_REC, 0x00020000);
        const auto rq = __builtin_amdgcn_make_buffer_rsrc((void*)pq_, 0, 4 * QTILE_REC, 0x00020000);
        const auto rqe = __builtin_amdgcn_make_buffer_rsrc((void*)pqe, 0, 4 * 512, 0x00020000);
        const auto rke = __builtin_amdgcn_make_buffer_rsrc((void*)pke, 0, 64 * 512, 0x00020000);
        struct KV { bf16x8 km; unsigned ke; bf16x8 v0, v1; };
        auto tileoff = [&](int blk) { return (unsigned)((4 * (blk & 15) + kq) * TILE_REC); };
        auto loadK = [&](KV& kv, int blk) { const unsigned so = tileoff(blk);
            kv.km = __builtin_bit_cast(bf16x8, __builtin_amdgcn_raw_buffer_load_b128(rkv, offK, so, SC1));
            kv.ke = __builtin_amdgcn_raw_buffer_load_b32(rke, offE, (4 * (blk & 15) + kq) * 512, SC1); };
        auto loadV0 = [&](KV& kv, int blk) { kv.v0 = __builtin_bit_cast(bf16x8, __builtin_amdgcn_raw_buffer_load_b128(rkv, offV0, tileoff(blk), SC1)); };
        auto loadV1 = [&](KV& kv, int blk) { kv.v1 = __builtin_bit_cast(bf16x8, __builtin_amdgcn_raw_buffer_load_b128(rkv, offV1, tileoff(blk), SC1)); };
        KV A, B;
        bf16x8 qm[4]; s16x4 qe[4];
        auto qblk = [&](int i) { return (kq + i) & 3; };
        auto loadQ = [&](int i) {
            qm[i] = __builtin_bit_cast(bf16x8, __builtin_amdgcn_raw_buffer_load_b128(rq, offK, qblk(i) * QTILE_REC, SC1));
            const u32x2 t = {__builtin_amdgcn_raw_buffer_load_b32(rqe, offEq, qblk(i) * 512, SC1), 0u}; qe[i] = __builtin_bit_cast(s16x4, t); };
        const u32x2 gw2 = __builtin_amdgcn_raw_buffer_load_b64(__builtin_amdgcn_make_buffer_rsrc((void*)(a.ws + WS_GR + (xcd & 3) * 384), 0, 384, 0x00020000), have2 ? 0x80000000u : (lane < 48 ? lane : lane - 16) * 8, 0, SC1);
        FENCE();
        loadK(A, j); loadQ(0); SBAR(); loadQ(1); loadV0(A, j); SBAR(); loadQ(2); loadV1(A, j); SBAR(); loadQ(3); loadK(B, j + 1); loadV0(B, j + 1);
        FENCE();
#pragma unroll
        for (int qb = 0; qb < 4; ++qb) {
#pragma unroll
            for (int i = 0; i < 16; ++i) O[qb][i] = 0.f;
            PIN(O[qb]);
        }
        if (cv_on) { __builtin_amdgcn_s_setprio(3); conv_finish(lds, wid & 1, cv_pair, cv_lu, cv_lx, lane, cm, zp); __builtin_amdgcn_s_setprio(0); }
        FENCE();
        auto kext = [&](const KV& kv) { const u32x2 t = {kv.ke, 0x3f803f80u}; return __builtin_bit_cast(s16x4, t); };
        f32x16 zero; for (int i = 0; i < 16; ++i) zero[i] = 0.f;
        if (poller) {
            unsigned nv = have2 ? (unsigned)gv : gw2[0];
            if (__builtin_expect(!have2 && !__all(!need2 || gw2[1] == tag), 0)) {
#pragma unroll 1
                for (unsigned spin = 0; spin < SPIN_MAX; ++spin) {
                    gv = ld_gran(gp);
                    if (__all(!need2 || (unsigned)(gv >> 32) == tag)) break;
                    __builtin_amdgcn_s_sleep(QSLEEP2);
                }
                nv = (unsigned)gv;
            }
            nv = need2 ? nv : 0u;
            nv = max(nv, (unsigned)__builtin_amdgcn_update_dpp(0, (int)nv, 0x111, 0xf, 0xf, false));
            nv = max(nv, (unsigned)__builtin_amdgcn_update_dpp(0, (int)nv, 0x112, 0xf, 0xf, false));
            nv = max(nv, (unsigned)__builtin_amdgcn_update_dpp(0, (int)nv, 0x114, 0xf, 0xf, false));
            nv = max(nv, (unsigned)__builtin_amdgcn_update_dpp(0, (int)nv, 0x118, 0xf, 0xf, false));
            const float bq = 3.f * __uint_as_float(__builtin_amdgcn_readlane((int)nv, 15)), bk = 3.f * __uint_as_float(__builtin_amdgcn_readlane((int)nv, 31)), bv = __uint_as_float(__builtin_amdgcn_readlane((int)nv, 47));
            lds_set(lds, SY_QKV, ((bq * bk <= 4096.f) && (bv <= 1.2e24f)) ? 1u : 2u);
        }
        const bool safe = lds_wait(lds, SY_QKV, 1) == 1u;
        {
            if (!safe) {
                float mx[4] = {-3.0e38f, -3.0e38f, -3.0e38f, -3.0e38f};
#pragma unroll 1
                for (int s = 0; s < 16; ++s) {
                    KV T; loadK(T, s);
                    const s16x4 ke = kext(T);
#pragma unroll
                    for (int qb = 0; qb < 4; ++qb) {
                        f32x16 Sx = MFMA16(T.km, qm[qb], zero); Sx = MFMA8(ke, qe[qb], Sx);
#pragma unroll
                        for (int i = 0; i < 16; ++i) mx[qb] = fmaxf(mx[qb], Sx[i]);
                    }
                }
#pragma unroll
                for (int qb = 0; qb < 4; ++qb) { mx[qb] = fmaxf(mx[qb], __shfl_xor(mx[qb], 32));
                    if (hi == 0) *(LAS float*)(lds + L_MX + ((hd * 4 + kq) * 128 + qblk(qb) * 32 + r32) * 4) = mx[qb]; }
                BARRIER();
#pragma unroll
                for (int qb = 0; qb < 4; ++qb) {
                    float m = -3.0e38f;
#pragma unroll
                    for (int w = 0; w < 4; ++w) m = fmaxf(m, *(const LAS float*)(lds + L_MX + ((hd * 4 + w) * 128 + qblk(qb) * 32 + r32) * 4));
                    const unsigned h1 = cvtpk(-m, 0.f) & 0xffffu; const float r1 = -m - bflo(h1);
                    const unsigned h2 = cvtpk(r1, 0.f) & 0xffffu; const float r2 = r1 - bflo(h2);
                    const unsigned h3 = cvtpk(r2, 0.f) & 0xffffu;
                    u32x2 t = __builtin_bit_cast(u32x2, qe[qb]); t[1] = hi ? h3 : (h1 | (h2 << 16)); qe[qb] = __builtin_bit_cast(s16x4, t);
                }
            }
        }
        f32x16 S0, S1;
        s16x4 keA = kext(A);
        S0 = MFMA16(A.km, qm[0], zero); S0 = MFMA8(keA, qe[0], S0);
        float T0, T1, T2, T3; u32x4 p0, p1;
        T0 = T1 = T2 = T3 = 0.f; p1 = (u32x4){0u, 0u, 0u, 0u}; p0 = p1;
#define EX4(Sc, i) do { T0 = __builtin_amdgcn_exp2f(Sc[i]); SBAR(); T1 = __builtin_amdgcn_exp2f(Sc[i + 1]); SBAR(); T2 = __builtin_amdgcn_exp2f(Sc[i + 2]); SBAR(); T3 = __builtin_amdgcn_exp2f(Sc[i + 3]); PIN(T0); PIN(T1); PIN(T2); PIN(T3); SBAR(); } while (0)
#define CV2(P, i) do { P[i] = cvtpk(T0, T1); SBAR(); P[i + 1] = cvtpk(T2, T3); PIN(P); SBAR(); } while (0)
#define TILE(Sc, Sn, KM, KE, qn, V0, qb, PV1, qp) do { \
        Sn = MFMA16(KM, qm[qn], zero); SBAR(); \
        CV2(p1, 2); EX4(Sc, 0); \
        Sn = MFMA8(KE, qe[qn], Sn); SBAR(); \
        CV2(p0, 0); EX4(Sc, 4); \
        O[qp] = MFMA16(PV1, __builtin_bit_cast(bf16x8, p1), O[qp]); SBAR(); \
        CV2(p0, 2); EX4(Sc, 8); \
        O[qb] = MFMA16(V0, __builtin_bit_cast(bf16x8, p0), O[qb]); SBAR(); \
        CV2(p1, 0); EX4(Sc, 12); \
    } while (0)
#define PR_LO(on) do { if (on) { if (!hd) __builtin_amdgcn_s_setprio(0); } } while (0)
#define PR_HI(on) do { if (on) { if (!hd) __builtin_amdgcn_s_setprio(2); } } while (0)
#define STEP(C, keC, N, keN, sb, second, EXTRA) do { \
        PR_LO(!(second)); \
        TILE(S0, S1, C.km, keC, 1, C.v0, 0, N.v1, 3); \
        loadV1(N, (sb) + 1); EXTRA; SBAR(); \
        TILE(S1, S0, C.km, keC, 2, C.v0, 1, C.v1, 0); \
        PR_HI(!(second)); \
        TILE(S0, S1, C.km, keC, 3, C.v0, 2, C.v1, 1); \
        loadK(C, (sb) + 2); SBAR(); \
        keN = kext(N); \
        TILE(S1, S0, N.km, keN, 0, C.v0, 3, C.v1, 2); \
        loadV0(C, (sb) + 2); SBAR(); \
    } while (0)
        B.v1 = (bf16x8){0, 0, 0, 0, 0, 0, 0, 0}; PIN(B.v1);
        s16x4 keB;
        PIN(S0);
        if (hd) __builtin_amdgcn_s_setprio(1);
        const auto rwo = __builtin_amdgcn_make_buffer_rsrc((void*)(a.wo + cg * 144), 0, D * D * 2 - cg * 144, 0x00020000);
        auto wdma = [&](unsigned v, int p, int k, int sp) { __builtin_amdgcn_raw_ptr_buffer_load_lds(rwo, (LAS void*)(lds + L_WO + (9 * p + k) * 1024), 16, v, sp * (72 * 1152), 0, 0); };
#pragma unroll 1
        for (int it = 0; it < 3; ++it) {
            STEP(A, keA, B, keB, j + 2 * it, 0, );
            STEP(B, keB, A, keA, j + 2 * it + 1, 1, );
        }
        if (cv_on) {
            const auto rz = __builtin_amdgcn_make_buffer_rsrc((void*)(a.ws + WS_Z + cv_ch * 4096 + (wid & 1) * 2048), 0, 2048, 0x00020000);
            __builtin_amdgcn_raw_buffer_store_b128(zp[0], rz, zo, 0, SC1); __builtin_amdgcn_raw_buffer_store_b128(zp[1], rz, zo, 1024, SC1);
        }
        __builtin_amdgcn_raw_buffer_store_b128((u32x4){POISON, POISON, POISON, POISON}, ro, oslot, 0, SC1);
        if (hd == 0) __builtin_amdgcn_raw_buffer_store_b64((u32x2){POISON, POISON}, ro, oslot_e, 0, SC1);
        lds_wait(lds, SY_CONVDONE, nconv);
        if (wid == 0) wdma(wvf, 8, 8, 0);
#pragma unroll 1
        for (int it = 3; it < 8; ++it) {
            if (it == 5) {
                WAITVM(16);
                if (lds_inc_ret(lds, SY_ZD) == 7u) { if (lane < 2) st_flag(a.ws + WS_FEND + fo, tag); }
            }
            STEP(A, keA, B, keB, j + 2 * it, 0, wdma(it < 7 ? wva : wve, 2 * it - 6, wid, it < 7 ? 2 * it - 6 : 0));
            STEP(B, keB, A, keA, j + 2 * it + 1, 1, wdma(it < 7 ? wva : wvb, it < 7 ? 2 * it - 5 : wid, it < 7 ? wid : 8, it < 7 ? 2 * it - 5 : wid));
        }
        __builtin_amdgcn_s_setprio(0);
        { CV2(p1, 2); O[3] = MFMA16(B.v1, __builtin_bit_cast(bf16x8, p1), O[3]); }
    }
    {
        if (wid == 4) fv = ld_flag(p3flag());
        const int rt = kq, kh = hd;
        const int l15 = lane & 15, g4 = lane >> 4, q4 = l15 >> 2, p4 = lane & 3;
        const int t0 = 64 * rh + 16 * rt;
        FENCE();
        const ldsp r2 = lds + (hi ? L_Z : L_RED2 + (hd * 16 + kq) * 512) + r32 * 16;
#pragma unroll
        for (int i = 0; i < 4; ++i) {
            const int qb = (kq + i) & 3;
            *(LAS f32x4*)(r2 + qb * 2048) = (f32x4){O[i][8], O[i][9], O[i][10], O[i][11]};
            if (i) { const ldsp p = lds + L_RED + ((hd * 4 + qb) * 3 + 3 - i) * 2048 + lane * 16;
                *(LAS f32x4*)p = (f32x4){O[i][0], O[i][1], O[i][2], O[i][3]}; *(LAS f32x4*)(p + 1024) = (f32x4){O[i][4], O[i][5], O[i][6], O[i][7]}; }
        }
        f32x4 a0 = {O[0][0], O[0][1], O[0][2], O[0][3]}, a1 = {O[0][4], O[0][5], O[0][6], O[0][7]};
        if (wid == 4) WAITVM(4); else WAITVM(3);
        BARRIER();
        unsigned zo0, zo1, go, wpo, o16, so;
        const char *zb, *gb;
        {
            f32x4 pa[3], pb[3], pc[4], pd[4];
#pragma unroll
            for (int w = 0; w < 3; ++w) { const ldsp p = lds + L_RED + ((hd * 4 + kq) * 3 + w) * 2048 + lane * 16; pa[w] = *(const LAS f32x4*)p; pb[w] = *(const LAS f32x4*)(p + 1024); }
#pragma unroll
            for (int w = 0; w < 4; ++w) pc[w] = *(const LAS f32x4*)(lds + L_RED2 + ((hd * 4 + kq) * 4 + w) * 512 + r32 * 16);
#pragma unroll
            for (int w = 0; w < 4; ++w) pd[w] = *(const LAS f32x4*)(lds + L_RED2 + ((4 + kq) * 4 + w) * 512 + r32 * 16);
            SBAR();
            a0 = ((a0 + pa[0]) + pa[1]) + pa[2]; a1 = ((a1 + pb[0]) + pb[1]) + pb[2];
            const f32x4 a2 = ((pc[0] + pc[1]) + pc[2]) + pc[3];
            const float inv = __builtin_amdgcn_rcpf(a2[2]);
            auto pk = [](float lo, float hh) { const unsigned u = cvtpk(lo, hh); return u == POISON ? POISON - 1u : u; };
            const u32x4 om = {pk(a0[0] * inv, a0[1] * inv), pk(a0[2] * inv, a0[3] * inv), pk(a1[0] * inv, a1[1] * inv), pk(a1[2] * inv, a1[3] * inv)};
            __builtin_amdgcn_raw_buffer_store_b128(om, ro, oslot, 0, SC1);
            if (hd == 0) {
                const f32x4 b2 = ((pd[0] + pd[1]) + pd[2]) + pd[3];
                const float inv2 = __builtin_amdgcn_rcpf(b2[2]);
                const u32x2 oe = {pk(a2[0] * inv, a2[1] * inv), pk(b2[0] * inv2, b2[1] * inv2)};
                __builtin_amdgcn_raw_buffer_store_b64(oe, ro, oslot_e, 0, SC1);
            }
            FENCE();
        }
        const u32x4 ef = __builtin_amdgcn_raw_buffer_load_b128(__builtin_amdgcn_make_buffer_rsrc((void*)(a.ws + WS_FEND), 0, 1024, 0x00020000), b == 0 && wid == 0 ? lane * 16 : 0x80000000u, 0, SC1);
        FENCE();
        if (wid == 4) {
            PIN(fv);
            if (!__all(fv == tag)) poll_flags(p3flag(), true, tag);
            lds_set(lds, SY_P3, 1);
        }
        zo0 = (8 * wid + (lane >> 3)) * 4096 + (lane & 7) * 16; zo1 = wid == 0 ? (64 + (lane >> 3)) * 4096 + (lane & 7) * 16 : 0x80000000u; go = (t0 + l15) * 4;
        wpo = L_WO + (32 * 9 * kh + 8 * g4 + 2 * q4) * 144 + p4 * 8; o16 = lane * 16;
        zb = a.ws + WS_Z + 72 * cg * 4096 + 128 * rh; gb = a.ws + WS_GATE; so = ((4 * rh + rt) * 18 + 9 * kh) * 1024;
        asm volatile("" : "+s"(zb), "+s"(gb), "+s"(so)); PIN(zo0); PIN(zo1); PIN(go); PIN(wpo); PIN(o16);
        lds_wait(lds, SY_P3, 1);
        const auto rz = __builtin_amdgcn_make_buffer_rsrc((void*)zb, 0, 72 * 4096, 0x00020000);
        const auto rg = __builtin_amdgcn_make_buffer_rsrc((void*)gb, 0, S * 4, 0x00020000);
        u32x4 zr[2];
        zr[0] = __builtin_amdgcn_raw_buffer_load_b128(rz, zo0, 0, SC1);
        zr[1] = __builtin_amdgcn_raw_buffer_load_b128(rz, zo1, 0, SC1);
        const float logit = __uint_as_float(__builtin_amdgcn_raw_buffer_load_b32(rg, go, 0, SC1));
        FENCE();
        bf16x8 of[9];
        auto load_o = [&](int i) { of[i] = __builtin_bit_cast(bf16x8, __builtin_amdgcn_raw_buffer_load_b128(ro, o16, so + i * 1024, SC1)); };
        s16x4 wl[9][5], wh[9][5];
        const ldsp wp = lds + wpo;
        auto load_w = [&](int i) {
#pragma unroll
            for (int m = 0; m < 5; ++m) { wl[i][m] = trread(wp + i * 32 * 144 + m * 32); wh[i][m] = trread(wp + i * 32 * 144 + 144 + m * 32); } };
#pragma unroll
        for (int i = 0; i < 9; ++i) { load_o(i); SBAR(); load_w(i); SBAR(); }
        *(LAS u32x4*)(lds + L_Z + wid * 1024 + lane * 16) = zr[0];
        if (wid == 0) *(LAS u32x4*)(lds + L_Z + 8192 + lane * 16) = zr[1];
        WAITLDS();
        lds_inc(lds, SY_ZS);
        const float gt = __builtin_amdgcn_rcpf(1.f + __expf(-logit)), gn = 1.f - gt;
        FENCE();
        f32x4 acc[5];
#pragma unroll
        for (int m = 0; m < 5; ++m) acc[m] = (f32x4){0.f, 0.f, 0.f, 0.f};
        auto unset = [&](int i) { const u32x4 u = __builtin_bit_cast(u32x4, of[i]); return __any(max(max(u[0], u[1]), max(u[2], u[3])) == POISON); };
#pragma unroll
        for (int i = 0; i < 9; ++i) {
            if (__builtin_expect(unset(i), 0)) {
#pragma unroll 1
                for (unsigned spin = 0; spin < SPIN_MAX; ++spin) {
                    __builtin_amdgcn_s_sleep(OSLEEP);
                    FENCE();
#pragma unroll
                    for (int n = i; n < 9; ++n) if (n == i || unset(n)) load_o(n);
                    __builtin_amdgcn_s_waitcnt(0x0f70);
                    FENCE();
                    if (!unset(i)) break;
                }
            }
#pragma unroll
            for (int m = 0; m < 5; ++m) acc[m] = MFMAS(__builtin_shufflevector(wl[i][m], wh[i][m], 0, 1, 2, 3, 4, 5, 6, 7), of[i], acc[m]);
            SBAR();
        }
        const ldsp exw = lds + L_RED + wid * 6144 + lane * 16, exr = lds + L_RED + (wid ^ 4) * 6144 + lane * 16;
        const auto rout = __builtin_amdgcn_make_buffer_rsrc((void*)a.out, 0, S * D * 2, 0x00020000);
        auto finish = [&](auto khc) {
            constexpr int KH = decltype(khc)::value, NT = KH ? 2 : 3;
#pragma unroll
            for (int m = 0; m < 5; ++m) if ((m >= 3) != (KH == 1)) *(LAS f32x4*)(exw + m * 1024) = acc[m];
            WAITLDS();
            lds_inc(lds, SY_X3 + rt);
            lds_wait(lds, SY_ZS, 8);
            lds_wait(lds, SY_X3 + rt, 2);
#pragma unroll
            for (int i = 0; i < NT; ++i) {
                const int m = 3 * KH + i;
                const f32x4 y = acc[m] + *(const LAS f32x4*)(exr + m * 1024);
                const u32x2 z = __builtin_bit_cast(u32x2, trread(lds + L_Z + (16 * m + 4 * g4 + q4) * 128 + rt * 32 + p4 * 8));
                const u32x2 xx = xv[i];
                const float o0 = gt * y[0] + gn * bflo(xx[0]) * bflo(z[0]), o1 = gt * y[1] + gn * bfhi(xx[0]) * bfhi(z[0]);
                const float o2 = gt * y[2] + gn * bflo(xx[1]) * bflo(z[1]), o3 = gt * y[3] + gn * bfhi(xx[1]) * bfhi(z[1]);
                __builtin_amdgcn_raw_buffer_store_b64((u32x2){cvtpk(o0, o1), cvtpk(o2, o3)}, rout, (m < 4 || g4 < 2) ? xo + i * 32 : 0x80000000u, 0, 0);
            }
        };
        if (kh) finish(std::integral_constant<int, 1>{}); else finish(std::integral_constant<int, 0>{});
        if (b == 0 && wid == 0) {
            if (!__all(ef[0] == tag && ef[1] == tag && ef[2] == tag && ef[3] == tag)) {
#pragma unroll 1
                for (int n = 0; n < 4; ++n) poll_flags(a.ws + WS_FEND + (64 * n + lane) * 4, true, tag);
            }
            if (lane == 0) st_flag(a.ws + WS_E, tag);
        }
    }
}

extern "C" void kernel_launch(void* const* d_in, const int* in_sizes, int n_in, void* d_out, int out_size, void* d_ws, size_t ws_size, hipStream_t stream) {
    Args a;
    a.x = (const char*)d_in[0]; a.wqkv = (const char*)d_in[1]; a.wo = (const char*)d_in[2];
    a.bk = (const char*)d_in[4]; a.bv = (const char*)d_in[5]; a.shk = (const char*)d_in[6];
    a.ssmk = (const char*)d_in[7]; a.ssmd = (const char*)d_in[8]; a.gw = (const char*)d_in[9];
    a.out = (char*)d_out; a.ws = (char*)d_ws;
    (void)hipFuncSetAttribute((const void*)gated_mix_fused, hipFuncAttributeMaxDynamicSharedMemorySize, 163840);
    gated_mix_fused<<<256, 512, 163840, stream>>>(a);
}
```
